```python
import torch

def _softmax(x, axis=-1):
    return torch.softmax(x, dim=axis)
def _gelu(x):
    return torch.nn.functional.gelu(x, approximate="tanh")
def _silu(x):
    return torch.nn.functional.silu(x)
def _sigmoid(x):
    return torch.sigmoid(x)
def _layernorm(x, axis=-1, eps=1e-5):
    m = x.mean(dim=axis, keepdim=True)
    v = x.var(dim=axis, keepdim=True, unbiased=False)
    return (x - m) / torch.sqrt(v + eps)
def _scatter_add(tgt, idx, src, axis=0):
    return tgt.clone().index_add_(axis, idx, src)
def _segment_sum(src, seg, n):
    out = torch.zeros((n,) + src.shape[1:], dtype=src.dtype)
    return out.index_add_(0, seg, src)
def _pad(x, pw):
    flat = tuple(v for pair in reversed(pw) for v in pair)
    return torch.nn.functional.pad(x, flat)

kernel_name = 'sequential_preLN__spectral_gfnet__geglu_ffn__prompt'

def setup_inputs():

    _w = (lambda a: (lambda t: t.to(torch.bfloat16) if t.is_floating_point() else t)(torch.as_tensor(a)))
    def _A_setup():
        B, S, D = 1, 2048, 640
        Df = D // 2 + 1
        return {
            "x": _w((lambda *s: torch.randn(*s, dtype=torch.bfloat16) * 0.05)(B, S, D)),
            "w_r": _w((lambda *s: torch.randn(*s, dtype=torch.bfloat16) * 0.4)(S, Df) * 0.02),
            "w_i": _w((lambda *s: torch.randn(*s, dtype=torch.bfloat16) * 0.4)(S, Df) * 0.02),
        }
    _A_in = _A_setup()

    def _B_setup():
        B, S, D, F = 1, 2048, 640, 1536
        return {
            "x": _w((lambda *s: torch.randn(*s, dtype=torch.bfloat16) * 0.05)(B, S, D)),
            "w_gate": _w((lambda *s: torch.randn(*s, dtype=torch.bfloat16) * 0.05)(D, F)),
            "w_up": _w((lambda *s: torch.randn(*s, dtype=torch.bfloat16) * 0.05)(D, F)),
            "w_down": _w((lambda *s: torch.randn(*s, dtype=torch.bfloat16) * 0.05)(F, D)),
        }
    _B_in = _B_setup()

    return {
        "A_x": _A_in["x"],
        "A_w_r": _A_in["w_r"],
        "A_w_i": _A_in["w_i"],
        "B_x": _B_in["x"],
        "B_w_gate": _B_in["w_gate"],
        "B_w_up": _B_in["w_up"],
        "B_w_down": _B_in["w_down"],
    }

def reference(A_x, A_w_r, A_w_i, B_x, B_w_gate, B_w_up, B_w_down):
    def _A_ref(x, w_r, w_i):
        B, S, D = x.shape
        X = (lambda x, axes=(-2,-1): torch.fft.rfft2(x.float(), dim=axes))(x, axes=(-2, -1))
        W = w_r + 1j * w_i
        X = X * W
        y = (lambda x, s, axes=(-2,-1): torch.fft.irfft2(x, s=s, dim=axes).to(torch.bfloat16))(X, (S, D), axes=(-2, -1))
        return y

    def _B_ref(x, w_gate, w_up, w_down):
        return (_gelu(x @ w_gate) * (x @ w_up)) @ w_down

    x = A_x
    h = x + _A_ref(_layernorm(x, axis=-1), A_w_r, A_w_i)
    h2 = h + _B_ref(_layernorm(h, axis=-1), B_w_gate, B_w_up, B_w_down)
    return h2
```

```cpp
#include <hip/hip_runtime.h>
#include <hip/hip_bf16.h>
#include <stdint.h>
#include <type_traits>

typedef float f2 __attribute__((ext_vector_type(2)));
typedef float f4 __attribute__((ext_vector_type(4)));
typedef unsigned u4 __attribute__((ext_vector_type(4)));
typedef unsigned u2 __attribute__((ext_vector_type(2)));
typedef unsigned u4u __attribute__((ext_vector_type(4), aligned(2)));
typedef __bf16 bf2v __attribute__((ext_vector_type(2)));

constexpr size_t G_BYTES = (size_t)161 * 256 * 16 * 8;
constexpr size_t OFF_G1 = 0;
constexpr size_t OFF_G2 = OFF_G1 + G_BYTES;
constexpr size_t OFF_WT = OFF_G2 + G_BYTES;
constexpr size_t OFF_WS = OFF_WT + (size_t)161 * 16384;
constexpr size_t OFF_W1 = OFF_WT + G_BYTES;
constexpr size_t OFF_W2 = OFF_W1 + (size_t)16 * 4 * 20 * 3 * 1024;
constexpr size_t OFF_AF = OFF_W2 + (size_t)40 * 48 * 1024;
constexpr size_t OFF_H = OFF_AF + (size_t)128 * 20 * 1024;
constexpr size_t OFF_ACT = OFF_H + (size_t)2048 * 640 * 2;
constexpr size_t OFF_FL = OFF_ACT + (size_t)128 * 48 * 1024;
constexpr size_t OFF_DBG = (size_t)64 << 20;
constexpr int G2S = 161 * 16;

#define STAMP_DECL
#define STAMP(i)
#define STAMP_OUT(dbg, kid)
#define DBG_ARG
#define DBG_PASS

template <int SW, int NL, int NH> __device__ __forceinline__ f2 pk_add(f2 a, f2 b) {
  f2 r;
  asm("v_pk_add_f32 %0, %1, %2 op_sel:[0,%c3] op_sel_hi:[1,%c4] neg_lo:[0,%c5] neg_hi:[0,%c6]"
      : "=v"(r) : "v"(a), "v"(b), "n"(SW), "n"(1 - SW), "n"(NL), "n"(NH));
  return r;
}
__device__ __forceinline__ f2 padd(f2 a, f2 b) { return pk_add<0, 0, 0>(a, b); }
__device__ __forceinline__ f2 psub(f2 a, f2 b) { return pk_add<0, 1, 1>(a, b); }
template <int DIR> __device__ __forceinline__ f2 addrot(f2 a, f2 b) { return pk_add<1, (DIR < 0), (DIR > 0)>(a, b); }
template <int DIR> __device__ __forceinline__ f2 subrot(f2 a, f2 b) { return pk_add<1, (DIR > 0), (DIR < 0)>(a, b); }
template <int SW, int KS, int NL, int NH, int CW = 0, int CNL = 0, int CNH = 0>
__device__ __forceinline__ f2 pk_fma_s(f2 b, f2 k, f2 c) {
  f2 r;
  asm("v_pk_fma_f32 %0, %1, %2, %3 op_sel:[%c4,%c6,%c9] op_sel_hi:[%c5,%c6,%c10] neg_lo:[%c7,0,%c11] neg_hi:[%c8,0,%c12]"
      : "=v"(r) : "v"(b), "s"(k), "v"(c), "n"(SW), "n"(1 - SW), "n"(KS), "n"(NL), "n"(NH), "n"(CW), "n"(1 - CW), "n"(CNL), "n"(CNH));
  return r;
}
template <int KS> __device__ __forceinline__ f2 fmak(f2 b, f2 k, f2 c) { return pk_fma_s<0, KS, 0, 0>(b, k, c); }
template <int KS> __device__ __forceinline__ f2 fmsk(f2 b, f2 k, f2 c) { return pk_fma_s<0, KS, 1, 1>(b, k, c); }
template <int DIR, int KS> __device__ __forceinline__ f2 fmarot(f2 b, f2 k, f2 c) { return pk_fma_s<1, KS, (DIR < 0), (DIR > 0)>(b, k, c); }
template <int DIR, int KS> __device__ __forceinline__ f2 fmsrot(f2 b, f2 k, f2 c) { return pk_fma_s<1, KS, (DIR > 0), (DIR < 0)>(b, k, c); }
template <int DIR, int KS> __device__ __forceinline__ f2 rotfma(f2 b, f2 k) { return pk_fma_s<0, KS, 0, 0, 1, (DIR < 0), (DIR > 0)>(b, k, b); }
template <int A0, int A1, int B0, int B1, int NL, int NH> __device__ __forceinline__ f2 pk_mul_v(f2 a, f2 b) {
  f2 r;
  asm("v_pk_mul_f32 %0, %1, %2 op_sel:[%c3,%c5] op_sel_hi:[%c4,%c6] neg_lo:[%c7,0] neg_hi:[%c8,0]"
      : "=v"(r) : "v"(a), "v"(b), "n"(A0), "n"(A1), "n"(B0), "n"(B1), "n"(NL), "n"(NH));
  return r;
}
template <int A0, int A1, int B0, int B1, int NL, int NH> __device__ __forceinline__ f2 pk_fma_v(f2 a, f2 b, f2 c) {
  f2 r;
  asm("v_pk_fma_f32 %0, %1, %2, %3 op_sel:[%c4,%c6,0] op_sel_hi:[%c5,%c7,1] neg_lo:[%c8,0,0] neg_hi:[%c9,0,0]"
      : "=v"(r) : "v"(a), "v"(b), "v"(c), "n"(A0), "n"(A1), "n"(B0), "n"(B1), "n"(NL), "n"(NH));
  return r;
}
__device__ __forceinline__ f2 cmul(f2 a, f2 w) { return pk_fma_v<1, 1, 1, 0, 1, 0>(a, w, pk_mul_v<0, 0, 0, 1, 0, 0>(a, w)); }
__device__ __forceinline__ f2 cmulc(f2 a, f2 w) { return pk_fma_v<1, 1, 1, 0, 0, 0>(a, w, pk_mul_v<0, 0, 0, 1, 0, 1>(a, w)); }
__device__ __forceinline__ f2 cmac(f2 a, f2 w, f2 c) { return pk_fma_v<1, 1, 1, 0, 1, 0>(a, w, pk_fma_v<0, 0, 0, 1, 0, 0>(a, w, c)); }
__device__ __forceinline__ f2 cmscc(f2 a, f2 w, f2 c) { return pk_fma_v<1, 1, 1, 0, 1, 1>(a, w, pk_fma_v<0, 0, 0, 1, 1, 0>(a, w, c)); }

#define K_RC (f2{0.70710678118654752440f, 0.92387953251128675613f})
#define K_T8 (f2{0.41421356237309504880f, 0.f})
#define K_C5 (f2{0.30901699437494742410f, -0.80901699437494742410f})
#define K_S5 (f2{0.95105651629515357212f, 0.61803398874989484820f})
__device__ __forceinline__ void fft16_f(f2 (&x)[16]) {
  f2 t0, t1, t2, t3;
  asm(
    "v_pk_add_f32 %16, %0, %8\n\t"
    "v_pk_add_f32 %18, %1, %9\n\t"
    "v_pk_add_f32 %8, %0, %8 neg_lo:[0,1] neg_hi:[0,1]\n\t"
    "v_pk_add_f32 %9, %1, %9 neg_lo:[0,1] neg_hi:[0,1]\n\t"
    "v_pk_add_f32 %17, %4, %12\n\t"
    "v_pk_add_f32 %19, %5, %13\n\t"
    "v_pk_add_f32 %12, %4, %12 neg_lo:[0,1] neg_hi:[0,1]\n\t"
    "v_pk_add_f32 %13, %5, %13 neg_lo:[0,1] neg_hi:[0,1]\n\t"
    "v_pk_add_f32 %0, %16, %17\n\t"
    "v_pk_add_f32 %1, %18, %19\n\t"
    "v_pk_add_f32 %4, %8, %12 op_sel:[0,1] op_sel_hi:[1,0] neg_hi:[0,1]\n\t"
    "v_pk_add_f32 %5, %9, %13 op_sel:[0,1] op_sel_hi:[1,0] neg_hi:[0,1]\n\t"
    "v_pk_add_f32 %12, %8, %12 op_sel:[0,1] op_sel_hi:[1,0] neg_lo:[0,1]\n\t"
    "v_pk_add_f32 %13, %9, %13 op_sel:[0,1] op_sel_hi:[1,0] neg_lo:[0,1]\n\t"
    "v_pk_add_f32 %8, %16, %17 neg_lo:[0,1] neg_hi:[0,1]\n\t"
    "v_pk_add_f32 %9, %18, %19 neg_lo:[0,1] neg_hi:[0,1]\n\t"
    "v_pk_add_f32 %16, %2, %10\n\t"
    "v_pk_add_f32 %18, %3, %11\n\t"
    "v_pk_add_f32 %10, %2, %10 neg_lo:[0,1] neg_hi:[0,1]\n\t"
    "v_pk_add_f32 %11, %3, %11 neg_lo:[0,1] neg_hi:[0,1]\n\t"
    "v_pk_add_f32 %17, %6, %14\n\t"
    "v_pk_add_f32 %19, %7, %15\n\t"
    "v_pk_add_f32 %14, %6, %14 neg_lo:[0,1] neg_hi:[0,1]\n\t"
    "v_pk_add_f32 %15, %7, %15 neg_lo:[0,1] neg_hi:[0,1]\n\t"
    "v_pk_add_f32 %2, %16, %17\n\t"
    "v_pk_add_f32 %3, %18, %19\n\t"
    "v_pk_add_f32 %6, %10, %14 op_sel:[0,1] op_sel_hi:[1,0] neg_hi:[0,1]\n\t"
    "v_pk_add_f32 %7, %11, %15 op_sel:[0,1] op_sel_hi:[1,0] neg_hi:[0,1]\n\t"
    "v_pk_add_f32 %14, %10, %14 op_sel:[0,1] op_sel_hi:[1,0] neg_lo:[0,1]\n\t"
    "v_pk_add_f32 %15, %11, %15 op_sel:[0,1] op_sel_hi:[1,0] neg_lo:[0,1]\n\t"
    "v_pk_add_f32 %10, %16, %17 neg_lo:[0,1] neg_hi:[0,1]\n\t"
    "v_pk_add_f32 %11, %18, %19 neg_lo:[0,1] neg_hi:[0,1]\n\t"
    "v_pk_add_f32 %6, %6, %6 op_sel:[0,1] op_sel_hi:[1,0] neg_hi:[0,1]\n\t"
    "v_pk_add_f32 %14, %14, %14 op_sel:[0,1] op_sel_hi:[1,0] neg_lo:[0,1]\n\t"
    "v_pk_fma_f32 %5, %5, %21, %5 op_sel:[1,0,0] op_sel_hi:[0,0,1] neg_hi:[1,0,0]\n\t"
    "v_pk_fma_f32 %13, %13, %21, %13 op_sel:[0,0,1] op_sel_hi:[1,0,0] neg_hi:[0,0,1]\n\t"
    "v_pk_fma_f32 %7, %7, %21, %7 op_sel:[0,0,1] op_sel_hi:[1,0,0] neg_hi:[0,0,1]\n\t"
    "v_pk_fma_f32 %15, %15, %21, %15 op_sel:[1,0,0] op_sel_hi:[0,0,1] neg_hi:[1,0,0]\n\t"
    "v_pk_fma_f32 %16, %6, %20, %4 op_sel_hi:[1,0,1]\n\t"
    "v_pk_fma_f32 %18, %14, %20, %12 op_sel_hi:[1,0,1] neg_lo:[1,0,0] neg_hi:[1,0,0]\n\t"
    "v_pk_fma_f32 %6, %6, %20, %4 op_sel_hi:[1,0,1] neg_lo:[1,0,0] neg_hi:[1,0,0]\n\t"
    "v_pk_fma_f32 %14, %14, %20, %12 op_sel_hi:[1,0,1]\n\t"
    "v_pk_add_f32 %17, %5, %7\n\t"
    "v_pk_add_f32 %19, %13, %15 neg_lo:[0,1] neg_hi:[0,1]\n\t"
    "v_pk_add_f32 %7, %5, %7 neg_lo:[0,1] neg_hi:[0,1]\n\t"
    "v_pk_add_f32 %15, %13, %15\n\t"
    "v_pk_fma_f32 %4, %17, %20, %16 op_sel:[0,1,0]\n\t"
    "v_pk_fma_f32 %12, %19, %20, %18 op_sel:[0,1,0]\n\t"
    "v_pk_fma_f32 %5, %7, %20, %6 op_sel:[1,1,0] op_sel_hi:[0,1,1] neg_hi:[1,0,0]\n\t"
    "v_pk_fma_f32 %13, %15, %20, %14 op_sel:[1,1,0] op_sel_hi:[0,1,1] neg_hi:[1,0,0]\n\t"
    "v_pk_fma_f32 %7, %7, %20, %6 op_sel:[1,1,0] op_sel_hi:[0,1,1] neg_lo:[1,0,0]\n\t"
    "v_pk_fma_f32 %15, %15, %20, %14 op_sel:[1,1,0] op_sel_hi:[0,1,1] neg_lo:[1,0,0]\n\t"
    "v_pk_fma_f32 %6, %17, %20, %16 op_sel:[0,1,0] neg_lo:[1,0,0] neg_hi:[1,0,0]\n\t"
    "v_pk_fma_f32 %14, %19, %20, %18 op_sel:[0,1,0] neg_lo:[1,0,0] neg_hi:[1,0,0]\n\t"
    "v_pk_add_f32 %9, %9, %9 op_sel:[0,1] op_sel_hi:[1,0] neg_hi:[0,1]\n\t"
    "v_pk_add_f32 %18, %0, %2\n\t"
    "v_pk_add_f32 %11, %11, %11 op_sel:[0,1] op_sel_hi:[1,0] neg_lo:[0,1]\n\t"
    "v_pk_add_f32 %2, %0, %2 neg_lo:[0,1] neg_hi:[0,1]\n\t"
    "v_pk_add_f32 %16, %8, %10 op_sel:[0,1] op_sel_hi:[1,0] neg_hi:[0,1]\n\t"
    "v_pk_add_f32 %19, %1, %3\n\t"
    "v_pk_add_f32 %10, %8, %10 op_sel:[0,1] op_sel_hi:[1,0] neg_lo:[0,1]\n\t"
    "v_pk_add_f32 %3, %1, %3 neg_lo:[0,1] neg_hi:[0,1]\n\t"
    "v_pk_add_f32 %17, %9, %11 neg_lo:[0,1] neg_hi:[0,1]\n\t"
    "v_pk_add_f32 %0, %18, %19\n\t"
    "v_pk_add_f32 %11, %9, %11\n\t"
    "v_pk_add_f32 %1, %2, %3 op_sel:[0,1] op_sel_hi:[1,0] neg_hi:[0,1]\n\t"
    "v_pk_fma_f32 %8, %17, %20, %16 op_sel_hi:[1,0,1]\n\t"
    "v_pk_add_f32 %3, %2, %3 op_sel:[0,1] op_sel_hi:[1,0] neg_lo:[0,1]\n\t"
    "v_pk_fma_f32 %9, %11, %20, %10 op_sel:[1,0,0] op_sel_hi:[0,0,1] neg_hi:[1,0,0]\n\t"
    "v_pk_add_f32 %2, %18, %19 neg_lo:[0,1] neg_hi:[0,1]\n\t"
    "v_pk_fma_f32 %11, %11, %20, %10 op_sel:[1,0,0] op_sel_hi:[0,0,1] neg_lo:[1,0,0]\n\t"
    "v_pk_fma_f32 %10, %17, %20, %16 op_sel_hi:[1,0,1] neg_lo:[1,0,0] neg_hi:[1,0,0]\n\t"
    : "+v"(x[0]), "+v"(x[1]), "+v"(x[2]), "+v"(x[3]), "+v"(x[4]), "+v"(x[5]), "+v"(x[6]), "+v"(x[7]), "+v"(x[8]), "+v"(x[9]), "+v"(x[10]), "+v"(x[11]), "+v"(x[12]), "+v"(x[13]), "+v"(x[14]), "+v"(x[15]), "=&v"(t0), "=&v"(t1), "=&v"(t2), "=&v"(t3)
    : "s"(K_RC), "s"(K_T8));
  f2 y[16];
  y[0] = x[0];
  y[4] = x[1];
  y[8] = x[2];
  y[12] = x[3];
  y[1] = x[4];
  y[5] = x[5];
  y[9] = x[6];
  y[13] = x[7];
  y[2] = x[8];
  y[6] = x[9];
  y[10] = x[10];
  y[14] = x[11];
  y[3] = x[12];
  y[7] = x[13];
  y[11] = x[14];
  y[15] = x[15];
  x[0] = y[0];
  x[1] = y[1];
  x[2] = y[2];
  x[3] = y[3];
  x[4] = y[4];
  x[5] = y[5];
  x[6] = y[6];
  x[7] = y[7];
  x[8] = y[8];
  x[9] = y[9];
  x[10] = y[10];
  x[11] = y[11];
  x[12] = y[12];
  x[13] = y[13];
  x[14] = y[14];
  x[15] = y[15];
}
__device__ __forceinline__ void fft8_f(f2 (&x)[8]) {
  f2 t0, t1, t2, t3;
  asm(
    "v_pk_add_f32 %8, %0, %4\n\t"
    "v_pk_add_f32 %4, %0, %4 neg_lo:[0,1] neg_hi:[0,1]\n\t"
    "v_pk_add_f32 %9, %1, %5\n\t"
    "v_pk_add_f32 %5, %1, %5 neg_lo:[0,1] neg_hi:[0,1]\n\t"
    "v_pk_add_f32 %10, %2, %6\n\t"
    "v_pk_add_f32 %6, %2, %6 neg_lo:[0,1] neg_hi:[0,1]\n\t"
    "v_pk_add_f32 %11, %3, %7\n\t"
    "v_pk_add_f32 %7, %3, %7 neg_lo:[0,1] neg_hi:[0,1]\n\t"
    "v_pk_add_f32 %5, %5, %5 op_sel:[0,1] op_sel_hi:[1,0] neg_hi:[0,1]\n\t"
    "v_pk_add_f32 %2, %8, %10\n\t"
    "v_pk_add_f32 %7, %7, %7 op_sel:[0,1] op_sel_hi:[1,0] neg_lo:[0,1]\n\t"
    "v_pk_add_f32 %10, %8, %10 neg_lo:[0,1] neg_hi:[0,1]\n\t"
    "v_pk_add_f32 %0, %4, %6 op_sel:[0,1] op_sel_hi:[1,0] neg_hi:[0,1]\n\t"
    "v_pk_add_f32 %3, %9, %11\n\t"
    "v_pk_add_f32 %6, %4, %6 op_sel:[0,1] op_sel_hi:[1,0] neg_lo:[0,1]\n\t"
    "v_pk_add_f32 %11, %9, %11 neg_lo:[0,1] neg_hi:[0,1]\n\t"
    "v_pk_add_f32 %1, %5, %7 neg_lo:[0,1] neg_hi:[0,1]\n\t"
    "v_pk_add_f32 %8, %2, %3\n\t"
    "v_pk_add_f32 %7, %5, %7\n\t"
    "v_pk_add_f32 %9, %10, %11 op_sel:[0,1] op_sel_hi:[1,0] neg_hi:[0,1]\n\t"
    "v_pk_fma_f32 %4, %1, %12, %0 op_sel_hi:[1,0,1]\n\t"
    "v_pk_add_f32 %11, %10, %11 op_sel:[0,1] op_sel_hi:[1,0] neg_lo:[0,1]\n\t"
    "v_pk_fma_f32 %5, %7, %12, %6 op_sel:[1,0,0] op_sel_hi:[0,0,1] neg_hi:[1,0,0]\n\t"
    "v_pk_add_f32 %10, %2, %3 neg_lo:[0,1] neg_hi:[0,1]\n\t"
    "v_pk_fma_f32 %7, %7, %12, %6 op_sel:[1,0,0] op_sel_hi:[0,0,1] neg_lo:[1,0,0]\n\t"
    "v_pk_fma_f32 %6, %1, %12, %0 op_sel_hi:[1,0,1] neg_lo:[1,0,0] neg_hi:[1,0,0]\n\t"
    : "+v"(x[0]), "+v"(x[1]), "+v"(x[2]), "+v"(x[3]), "+v"(x[4]), "+v"(x[5]), "+v"(x[6]), "+v"(x[7]), "=&v"(t0), "=&v"(t1), "=&v"(t2), "=&v"(t3)
    : "s"(K_RC));
  x[1] = x[4]; x[3] = x[5]; x[5] = x[6]; x[0] = t0; x[2] = t1; x[4] = t2; x[6] = t3;
}
__device__ __forceinline__ void fft5_f(f2 (&x)[5]) {
  f2 t0, t1, t2, t3;
  asm(
    "v_pk_add_f32 %5, %1, %4\n\t"
    "v_pk_add_f32 %6, %2, %3\n\t"
    "v_pk_add_f32 %4, %1, %4 neg_lo:[0,1] neg_hi:[0,1]\n\t"
    "v_pk_add_f32 %3, %2, %3 neg_lo:[0,1] neg_hi:[0,1]\n\t"
    "v_pk_fma_f32 %7, %5, %9, %0 op_sel_hi:[1,0,1]\n\t"
    "v_pk_fma_f32 %8, %5, %9, %0 op_sel:[0,1,0]\n\t"
    "v_pk_add_f32 %0, %0, %5\n\t"
    "v_pk_fma_f32 %5, %3, %10, %4 op_sel:[0,1,0]\n\t"
    "v_pk_fma_f32 %3, %4, %10, %3 op_sel:[0,1,0] neg_lo:[0,0,1] neg_hi:[0,0,1]\n\t"
    "v_pk_fma_f32 %7, %6, %9, %7 op_sel:[0,1,0]\n\t"
    "v_pk_fma_f32 %8, %6, %9, %8 op_sel_hi:[1,0,1]\n\t"
    "v_pk_add_f32 %0, %0, %6\n\t"
    "v_pk_fma_f32 %1, %5, %10, %7 op_sel:[1,0,0] op_sel_hi:[0,0,1] neg_hi:[1,0,0]\n\t"
    "v_pk_fma_f32 %4, %5, %10, %7 op_sel:[1,0,0] op_sel_hi:[0,0,1] neg_lo:[1,0,0]\n\t"
    "v_pk_fma_f32 %2, %3, %10, %8 op_sel:[1,0,0] op_sel_hi:[0,0,1] neg_hi:[1,0,0]\n\t"
    "v_pk_fma_f32 %3, %3, %10, %8 op_sel:[1,0,0] op_sel_hi:[0,0,1] neg_lo:[1,0,0]\n\t"
    : "+v"(x[0]), "+v"(x[1]), "+v"(x[2]), "+v"(x[3]), "+v"(x[4]), "=&v"(t0), "=&v"(t1), "=&v"(t2), "=&v"(t3)
    : "s"(K_C5), "s"(K_S5));
}
__device__ __forceinline__ void tw4_f(f2* x, const f2* w) {
  f2 t0, t1, t2, t3;
  asm(
    "v_pk_mul_f32 %4, %0, %8 op_sel_hi:[0,1]\n\t"
    "v_pk_mul_f32 %5, %1, %9 op_sel_hi:[0,1]\n\t"
    "v_pk_mul_f32 %6, %2, %10 op_sel_hi:[0,1]\n\t"
    "v_pk_mul_f32 %7, %3, %11 op_sel_hi:[0,1]\n\t"
    "v_pk_fma_f32 %0, %0, %8, %4 op_sel:[1,1,0] op_sel_hi:[1,0,1] neg_lo:[1,0,0]\n\t"
    "v_pk_fma_f32 %1, %1, %9, %5 op_sel:[1,1,0] op_sel_hi:[1,0,1] neg_lo:[1,0,0]\n\t"
    "v_pk_fma_f32 %2, %2, %10, %6 op_sel:[1,1,0] op_sel_hi:[1,0,1] neg_lo:[1,0,0]\n\t"
    "v_pk_fma_f32 %3, %3, %11, %7 op_sel:[1,1,0] op_sel_hi:[1,0,1] neg_lo:[1,0,0]\n\t"
    : "+v"(x[0]), "+v"(x[1]), "+v"(x[2]), "+v"(x[3]), "=&v"(t0), "=&v"(t1), "=&v"(t2), "=&v"(t3)
    : "v"(w[0]), "v"(w[1]), "v"(w[2]), "v"(w[3]));
}
__device__ __forceinline__ void tw7_f(f2* x, const f2* w) {
  f2 t0, t1, t2, t3;
  asm(
    "v_pk_mul_f32 %7, %0, %11 op_sel_hi:[0,1]\n\t"
    "v_pk_mul_f32 %8, %1, %12 op_sel_hi:[0,1]\n\t"
    "v_pk_mul_f32 %9, %2, %13 op_sel_hi:[0,1]\n\t"
    "v_pk_mul_f32 %10, %3, %14 op_sel_hi:[0,1]\n\t"
    "v_pk_fma_f32 %0, %0, %11, %7 op_sel:[1,1,0] op_sel_hi:[1,0,1] neg_lo:[1,0,0]\n\t"
    "v_pk_fma_f32 %1, %1, %12, %8 op_sel:[1,1,0] op_sel_hi:[1,0,1] neg_lo:[1,0,0]\n\t"
    "v_pk_fma_f32 %2, %2, %13, %9 op_sel:[1,1,0] op_sel_hi:[1,0,1] neg_lo:[1,0,0]\n\t"
    "v_pk_fma_f32 %3, %3, %14, %10 op_sel:[1,1,0] op_sel_hi:[1,0,1] neg_lo:[1,0,0]\n\t"
    "v_pk_mul_f32 %7, %4, %15 op_sel_hi:[0,1]\n\t"
    "v_pk_mul_f32 %8, %5, %16 op_sel_hi:[0,1]\n\t"
    "v_pk_mul_f32 %9, %6, %17 op_sel_hi:[0,1]\n\t"
    "v_pk_fma_f32 %4, %4, %15, %7 op_sel:[1,1,0] op_sel_hi:[1,0,1] neg_lo:[1,0,0]\n\t"
    "v_pk_fma_f32 %5, %5, %16, %8 op_sel:[1,1,0] op_sel_hi:[1,0,1] neg_lo:[1,0,0]\n\t"
    "v_pk_fma_f32 %6, %6, %17, %9 op_sel:[1,1,0] op_sel_hi:[1,0,1] neg_lo:[1,0,0]\n\t"
    : "+v"(x[0]), "+v"(x[1]), "+v"(x[2]), "+v"(x[3]), "+v"(x[4]), "+v"(x[5]), "+v"(x[6]), "=&v"(t0), "=&v"(t1), "=&v"(t2), "=&v"(t3)
    : "v"(w[0]), "v"(w[1]), "v"(w[2]), "v"(w[3]), "v"(w[4]), "v"(w[5]), "v"(w[6]));
}
__device__ __forceinline__ void tw8_f(f2* x, const f2* w) {
  f2 t0, t1, t2, t3;
  asm(
    "v_pk_mul_f32 %8, %0, %12 op_sel_hi:[0,1]\n\t"
    "v_pk_mul_f32 %9, %1, %13 op_sel_hi:[0,1]\n\t"
    "v_pk_mul_f32 %10, %2, %14 op_sel_hi:[0,1]\n\t"
    "v_pk_mul_f32 %11, %3, %15 op_sel_hi:[0,1]\n\t"
    "v_pk_fma_f32 %0, %0, %12, %8 op_sel:[1,1,0] op_sel_hi:[1,0,1] neg_lo:[1,0,0]\n\t"
    "v_pk_fma_f32 %1, %1, %13, %9 op_sel:[1,1,0] op_sel_hi:[1,0,1] neg_lo:[1,0,0]\n\t"
    "v_pk_fma_f32 %2, %2, %14, %10 op_sel:[1,1,0] op_sel_hi:[1,0,1] neg_lo:[1,0,0]\n\t"
    "v_pk_fma_f32 %3, %3, %15, %11 op_sel:[1,1,0] op_sel_hi:[1,0,1] neg_lo:[1,0,0]\n\t"
    "v_pk_mul_f32 %8, %4, %16 op_sel_hi:[0,1]\n\t"
    "v_pk_mul_f32 %9, %5, %17 op_sel_hi:[0,1]\n\t"
    "v_pk_mul_f32 %10, %6, %18 op_sel_hi:[0,1]\n\t"
    "v_pk_mul_f32 %11, %7, %19 op_sel_hi:[0,1]\n\t"
    "v_pk_fma_f32 %4, %4, %16, %8 op_sel:[1,1,0] op_sel_hi:[1,0,1] neg_lo:[1,0,0]\n\t"
    "v_pk_fma_f32 %5, %5, %17, %9 op_sel:[1,1,0] op_sel_hi:[1,0,1] neg_lo:[1,0,0]\n\t"
    "v_pk_fma_f32 %6, %6, %18, %10 op_sel:[1,1,0] op_sel_hi:[1,0,1] neg_lo:[1,0,0]\n\t"
    "v_pk_fma_f32 %7, %7, %19, %11 op_sel:[1,1,0] op_sel_hi:[1,0,1] neg_lo:[1,0,0]\n\t"
    : "+v"(x[0]), "+v"(x[1]), "+v"(x[2]), "+v"(x[3]), "+v"(x[4]), "+v"(x[5]), "+v"(x[6]), "+v"(x[7]), "=&v"(t0), "=&v"(t1), "=&v"(t2), "=&v"(t3)
    : "v"(w[0]), "v"(w[1]), "v"(w[2]), "v"(w[3]), "v"(w[4]), "v"(w[5]), "v"(w[6]), "v"(w[7]));
}
__device__ __forceinline__ void fft16_i(f2 (&x)[16]) {
  f2 t0, t1, t2, t3;
  asm(
    "v_pk_add_f32 %16, %0, %8\n\t"
    "v_pk_add_f32 %18, %1, %9\n\t"
    "v_pk_add_f32 %8, %0, %8 neg_lo:[0,1] neg_hi:[0,1]\n\t"
    "v_pk_add_f32 %9, %1, %9 neg_lo:[0,1] neg_hi:[0,1]\n\t"
    "v_pk_add_f32 %17, %4, %12\n\t"
    "v_pk_add_f32 %19, %5, %13\n\t"
    "v_pk_add_f32 %12, %4, %12 neg_lo:[0,1] neg_hi:[0,1]\n\t"
    "v_pk_add_f32 %13, %5, %13 neg_lo:[0,1] neg_hi:[0,1]\n\t"
    "v_pk_add_f32 %0, %16, %17\n\t"
    "v_pk_add_f32 %1, %18, %19\n\t"
    "v_pk_add_f32 %4, %8, %12 op_sel:[0,1] op_sel_hi:[1,0] neg_lo:[0,1]\n\t"
    "v_pk_add_f32 %5, %9, %13 op_sel:[0,1] op_sel_hi:[1,0] neg_lo:[0,1]\n\t"
    "v_pk_add_f32 %12, %8, %12 op_sel:[0,1] op_sel_hi:[1,0] neg_hi:[0,1]\n\t"
    "v_pk_add_f32 %13, %9, %13 op_sel:[0,1] op_sel_hi:[1,0] neg_hi:[0,1]\n\t"
    "v_pk_add_f32 %8, %16, %17 neg_lo:[0,1] neg_hi:[0,1]\n\t"
    "v_pk_add_f32 %9, %18, %19 neg_lo:[0,1] neg_hi:[0,1]\n\t"
    "v_pk_add_f32 %16, %2, %10\n\t"
    "v_pk_add_f32 %18, %3, %11\n\t"
    "v_pk_add_f32 %10, %2, %10 neg_lo:[0,1] neg_hi:[0,1]\n\t"
    "v_pk_add_f32 %11, %3, %11 neg_lo:[0,1] neg_hi:[0,1]\n\t"
    "v_pk_add_f32 %17, %6, %14\n\t"
    "v_pk_add_f32 %19, %7, %15\n\t"
    "v_pk_add_f32 %14, %6, %14 neg_lo:[0,1] neg_hi:[0,1]\n\t"
    "v_pk_add_f32 %15, %7, %15 neg_lo:[0,1] neg_hi:[0,1]\n\t"
    "v_pk_add_f32 %2, %16, %17\n\t"
    "v_pk_add_f32 %3, %18, %19\n\t"
    "v_pk_add_f32 %6, %10, %14 op_sel:[0,1] op_sel_hi:[1,0] neg_lo:[0,1]\n\t"
    "v_pk_add_f32 %7, %11, %15 op_sel:[0,1] op_sel_hi:[1,0] neg_lo:[0,1]\n\t"
    "v_pk_add_f32 %14, %10, %14 op_sel:[0,1] op_sel_hi:[1,0] neg_hi:[0,1]\n\t"
    "v_pk_add_f32 %15, %11, %15 op_sel:[0,1] op_sel_hi:[1,0] neg_hi:[0,1]\n\t"
    "v_pk_add_f32 %10, %16, %17 neg_lo:[0,1] neg_hi:[0,1]\n\t"
    "v_pk_add_f32 %11, %18, %19 neg_lo:[0,1] neg_hi:[0,1]\n\t"
    "v_pk_add_f32 %6, %6, %6 op_sel:[0,1] op_sel_hi:[1,0] neg_lo:[0,1]\n\t"
    "v_pk_add_f32 %14, %14, %14 op_sel:[0,1] op_sel_hi:[1,0] neg_hi:[0,1]\n\t"
    "v_pk_fma_f32 %5, %5, %21, %5 op_sel:[1,0,0] op_sel_hi:[0,0,1] neg_lo:[1,0,0]\n\t"
    "v_pk_fma_f32 %13, %13, %21, %13 op_sel:[0,0,1] op_sel_hi:[1,0,0] neg_lo:[0,0,1]\n\t"
    "v_pk_fma_f32 %7, %7, %21, %7 op_sel:[0,0,1] op_sel_hi:[1,0,0] neg_lo:[0,0,1]\n\t"
    "v_pk_fma_f32 %15, %15, %21, %15 op_sel:[1,0,0] op_sel_hi:[0,0,1] neg_lo:[1,0,0]\n\t"
    "v_pk_fma_f32 %16, %6, %20, %4 op_sel_hi:[1,0,1]\n\t"
    "v_pk_fma_f32 %18, %14, %20, %12 op_sel_hi:[1,0,1] neg_lo:[1,0,0] neg_hi:[1,0,0]\n\t"
    "v_pk_fma_f32 %6, %6, %20, %4 op_sel_hi:[1,0,1] neg_lo:[1,0,0] neg_hi:[1,0,0]\n\t"
    "v_pk_fma_f32 %14, %14, %20, %12 op_sel_hi:[1,0,1]\n\t"
    "v_pk_add_f32 %17, %5, %7\n\t"
    "v_pk_add_f32 %19, %13, %15 neg_lo:[0,1] neg_hi:[0,1]\n\t"
    "v_pk_add_f32 %7, %5, %7 neg_lo:[0,1] neg_hi:[0,1]\n\t"
    "v_pk_add_f32 %15, %13, %15\n\t"
    "v_pk_fma_f32 %4, %17, %20, %16 op_sel:[0,1,0]\n\t"
    "v_pk_fma_f32 %12, %19, %20, %18 op_sel:[0,1,0]\n\t"
    "v_pk_fma_f32 %5, %7, %20, %6 op_sel:[1,1,0] op_sel_hi:[0,1,1] neg_lo:[1,0,0]\n\t"
    "v_pk_fma_f32 %13, %15, %20, %14 op_sel:[1,1,0] op_sel_hi:[0,1,1] neg_lo:[1,0,0]\n\t"
    "v_pk_fma_f32 %7, %7, %20, %6 op_sel:[1,1,0] op_sel_hi:[0,1,1] neg_hi:[1,0,0]\n\t"
    "v_pk_fma_f32 %15, %15, %20, %14 op_sel:[1,1,0] op_sel_hi:[0,1,1] neg_hi:[1,0,0]\n\t"
    "v_pk_fma_f32 %6, %17, %20, %16 op_sel:[0,1,0] neg_lo:[1,0,0] neg_hi:[1,0,0]\n\t"
    "v_pk_fma_f32 %14, %19, %20, %18 op_sel:[0,1,0] neg_lo:[1,0,0] neg_hi:[1,0,0]\n\t"
    "v_pk_add_f32 %9, %9, %9 op_sel:[0,1] op_sel_hi:[1,0] neg_lo:[0,1]\n\t"
    "v_pk_add_f32 %18, %0, %2\n\t"
    "v_pk_add_f32 %11, %11, %11 op_sel:[0,1] op_sel_hi:[1,0] neg_hi:[0,1]\n\t"
    "v_pk_add_f32 %2, %0, %2 neg_lo:[0,1] neg_hi:[0,1]\n\t"
    "v_pk_add_f32 %16, %8, %10 op_sel:[0,1] op_sel_hi:[1,0] neg_lo:[0,1]\n\t"
    "v_pk_add_f32 %19, %1, %3\n\t"
    "v_pk_add_f32 %10, %8, %10 op_sel:[0,1] op_sel_hi:[1,0] neg_hi:[0,1]\n\t"
    "v_pk_add_f32 %3, %1, %3 neg_lo:[0,1] neg_hi:[0,1]\n\t"
    "v_pk_add_f32 %17, %9, %11 neg_lo:[0,1] neg_hi:[0,1]\n\t"
    "v_pk_add_f32 %0, %18, %19\n\t"
    "v_pk_add_f32 %11, %9, %11\n\t"
    "v_pk_add_f32 %1, %2, %3 op_sel:[0,1] op_sel_hi:[1,0] neg_lo:[0,1]\n\t"
    "v_pk_fma_f32 %8, %17, %20, %16 op_sel_hi:[1,0,1]\n\t"
    "v_pk_add_f32 %3, %2, %3 op_sel:[0,1] op_sel_hi:[1,0] neg_hi:[0,1]\n\t"
    "v_pk_fma_f32 %9, %11, %20, %10 op_sel:[1,0,0] op_sel_hi:[0,0,1] neg_lo:[1,0,0]\n\t"
    "v_pk_add_f32 %2, %18, %19 neg_lo:[0,1] neg_hi:[0,1]\n\t"
    "v_pk_fma_f32 %11, %11, %20, %10 op_sel:[1,0,0] op_sel_hi:[0,0,1] neg_hi:[1,0,0]\n\t"
    "v_pk_fma_f32 %10, %17, %20, %16 op_sel_hi:[1,0,1] neg_lo:[1,0,0] neg_hi:[1,0,0]\n\t"
    : "+v"(x[0]), "+v"(x[1]), "+v"(x[2]), "+v"(x[3]), "+v"(x[4]), "+v"(x[5]), "+v"(x[6]), "+v"(x[7]), "+v"(x[8]), "+v"(x[9]), "+v"(x[10]), "+v"(x[11]), "+v"(x[12]), "+v"(x[13]), "+v"(x[14]), "+v"(x[15]), "=&v"(t0), "=&v"(t1), "=&v"(t2), "=&v"(t3)
    : "s"(K_RC), "s"(K_T8));
  f2 y[16];
  y[0] = x[0];
  y[4] = x[1];
  y[8] = x[2];
  y[12] = x[3];
  y[1] = x[4];
  y[5] = x[5];
  y[9] = x[6];
  y[13] = x[7];
  y[2] = x[8];
  y[6] = x[9];
  y[10] = x[10];
  y[14] = x[11];
  y[3] = x[12];
  y[7] = x[13];
  y[11] = x[14];
  y[15] = x[15];
  x[0] = y[0];
  x[1] = y[1];
  x[2] = y[2];
  x[3] = y[3];
  x[4] = y[4];
  x[5] = y[5];
  x[6] = y[6];
  x[7] = y[7];
  x[8] = y[8];
  x[9] = y[9];
  x[10] = y[10];
  x[11] = y[11];
  x[12] = y[12];
  x[13] = y[13];
  x[14] = y[14];
  x[15] = y[15];
}
__device__ __forceinline__ void fft8_i(f2 (&x)[8]) {
  f2 t0, t1, t2, t3;
  asm(
    "v_pk_add_f32 %8, %0, %4\n\t"
    "v_pk_add_f32 %4, %0, %4 neg_lo:[0,1] neg_hi:[0,1]\n\t"
    "v_pk_add_f32 %9, %1, %5\n\t"
    "v_pk_add_f32 %5, %1, %5 neg_lo:[0,1] neg_hi:[0,1]\n\t"
    "v_pk_add_f32 %10, %2, %6\n\t"
    "v_pk_add_f32 %6, %2, %6 neg_lo:[0,1] neg_hi:[0,1]\n\t"
    "v_pk_add_f32 %11, %3, %7\n\t"
    "v_pk_add_f32 %7, %3, %7 neg_lo:[0,1] neg_hi:[0,1]\n\t"
    "v_pk_add_f32 %5, %5, %5 op_sel:[0,1] op_sel_hi:[1,0] neg_lo:[0,1]\n\t"
    "v_pk_add_f32 %2, %8, %10\n\t"
    "v_pk_add_f32 %7, %7, %7 op_sel:[0,1] op_sel_hi:[1,0] neg_hi:[0,1]\n\t"
    "v_pk_add_f32 %10, %8, %10 neg_lo:[0,1] neg_hi:[0,1]\n\t"
    "v_pk_add_f32 %0, %4, %6 op_sel:[0,1] op_sel_hi:[1,0] neg_lo:[0,1]\n\t"
    "v_pk_add_f32 %3, %9, %11\n\t"
    "v_pk_add_f32 %6, %4, %6 op_sel:[0,1] op_sel_hi:[1,0] neg_hi:[0,1]\n\t"
    "v_pk_add_f32 %11, %9, %11 neg_lo:[0,1] neg_hi:[0,1]\n\t"
    "v_pk_add_f32 %1, %5, %7 neg_lo:[0,1] neg_hi:[0,1]\n\t"
    "v_pk_add_f32 %8, %2, %3\n\t"
    "v_pk_add_f32 %7, %5, %7\n\t"
    "v_pk_add_f32 %9, %10, %11 op_sel:[0,1] op_sel_hi:[1,0] neg_lo:[0,1]\n\t"
    "v_pk_fma_f32 %4, %1, %12, %0 op_sel_hi:[1,0,1]\n\t"
    "v_pk_add_f32 %11, %10, %11 op_sel:[0,1] op_sel_hi:[1,0] neg_hi:[0,1]\n\t"
    "v_pk_fma_f32 %5, %7, %12, %6 op_sel:[1,0,0] op_sel_hi:[0,0,1] neg_lo:[1,0,0]\n\t"
    "v_pk_add_f32 %10, %2, %3 neg_lo:[0,1] neg_hi:[0,1]\n\t"
    "v_pk_fma_f32 %7, %7, %12, %6 op_sel:[1,0,0] op_sel_hi:[0,0,1] neg_hi:[1,0,0]\n\t"
    "v_pk_fma_f32 %6, %1, %12, %0 op_sel_hi:[1,0,1] neg_lo:[1,0,0] neg_hi:[1,0,0]\n\t"
    : "+v"(x[0]), "+v"(x[1]), "+v"(x[2]), "+v"(x[3]), "+v"(x[4]), "+v"(x[5]), "+v"(x[6]), "+v"(x[7]), "=&v"(t0), "=&v"(t1), "=&v"(t2), "=&v"(t3)
    : "s"(K_RC));
  x[1] = x[4]; x[3] = x[5]; x[5] = x[6]; x[0] = t0; x[2] = t1; x[4] = t2; x[6] = t3;
}
__device__ __forceinline__ void fft5_i(f2 (&x)[5]) {
  f2 t0, t1, t2, t3;
  asm(
    "v_pk_add_f32 %5, %1, %4\n\t"
    "v_pk_add_f32 %6, %2, %3\n\t"
    "v_pk_add_f32 %4, %1, %4 neg_lo:[0,1] neg_hi:[0,1]\n\t"
    "v_pk_add_f32 %3, %2, %3 neg_lo:[0,1] neg_hi:[0,1]\n\t"
    "v_pk_fma_f32 %7, %5, %9, %0 op_sel_hi:[1,0,1]\n\t"
    "v_pk_fma_f32 %8, %5, %9, %0 op_sel:[0,1,0]\n\t"
    "v_pk_add_f32 %0, %0, %5\n\t"
    "v_pk_fma_f32 %5, %3, %10, %4 op_sel:[0,1,0]\n\t"
    "v_pk_fma_f32 %3, %4, %10, %3 op_sel:[0,1,0] neg_lo:[0,0,1] neg_hi:[0,0,1]\n\t"
    "v_pk_fma_f32 %7, %6, %9, %7 op_sel:[0,1,0]\n\t"
    "v_pk_fma_f32 %8, %6, %9, %8 op_sel_hi:[1,0,1]\n\t"
    "v_pk_add_f32 %0, %0, %6\n\t"
    "v_pk_fma_f32 %1, %5, %10, %7 op_sel:[1,0,0] op_sel_hi:[0,0,1] neg_lo:[1,0,0]\n\t"
    "v_pk_fma_f32 %4, %5, %10, %7 op_sel:[1,0,0] op_sel_hi:[0,0,1] neg_hi:[1,0,0]\n\t"
    "v_pk_fma_f32 %2, %3, %10, %8 op_sel:[1,0,0] op_sel_hi:[0,0,1] neg_lo:[1,0,0]\n\t"
    "v_pk_fma_f32 %3, %3, %10, %8 op_sel:[1,0,0] op_sel_hi:[0,0,1] neg_hi:[1,0,0]\n\t"
    : "+v"(x[0]), "+v"(x[1]), "+v"(x[2]), "+v"(x[3]), "+v"(x[4]), "=&v"(t0), "=&v"(t1), "=&v"(t2), "=&v"(t3)
    : "s"(K_C5), "s"(K_S5));
}
__device__ __forceinline__ void tw4_i(f2* x, const f2* w) {
  f2 t0, t1, t2, t3;
  asm(
    "v_pk_mul_f32 %4, %0, %8 op_sel_hi:[0,1] neg_hi:[1,0]\n\t"
    "v_pk_mul_f32 %5, %1, %9 op_sel_hi:[0,1] neg_hi:[1,0]\n\t"
    "v_pk_mul_f32 %6, %2, %10 op_sel_hi:[0,1] neg_hi:[1,0]\n\t"
    "v_pk_mul_f32 %7, %3, %11 op_sel_hi:[0,1] neg_hi:[1,0]\n\t"
    "v_pk_fma_f32 %0, %0, %8, %4 op_sel:[1,1,0] op_sel_hi:[1,0,1]\n\t"
    "v_pk_fma_f32 %1, %1, %9, %5 op_sel:[1,1,0] op_sel_hi:[1,0,1]\n\t"
    "v_pk_fma_f32 %2, %2, %10, %6 op_sel:[1,1,0] op_sel_hi:[1,0,1]\n\t"
    "v_pk_fma_f32 %3, %3, %11, %7 op_sel:[1,1,0] op_sel_hi:[1,0,1]\n\t"
    : "+v"(x[0]), "+v"(x[1]), "+v"(x[2]), "+v"(x[3]), "=&v"(t0), "=&v"(t1), "=&v"(t2), "=&v"(t3)
    : "v"(w[0]), "v"(w[1]), "v"(w[2]), "v"(w[3]));
}
__device__ __forceinline__ void tw7_i(f2* x, const f2* w) {
  f2 t0, t1, t2, t3;
  asm(
    "v_pk_mul_f32 %7, %0, %11 op_sel_hi:[0,1] neg_hi:[1,0]\n\t"
    "v_pk_mul_f32 %8, %1, %12 op_sel_hi:[0,1] neg_hi:[1,0]\n\t"
    "v_pk_mul_f32 %9, %2, %13 op_sel_hi:[0,1] neg_hi:[1,0]\n\t"
    "v_pk_mul_f32 %10, %3, %14 op_sel_hi:[0,1] neg_hi:[1,0]\n\t"
    "v_pk_fma_f32 %0, %0, %11, %7 op_sel:[1,1,0] op_sel_hi:[1,0,1]\n\t"
    "v_pk_fma_f32 %1, %1, %12, %8 op_sel:[1,1,0] op_sel_hi:[1,0,1]\n\t"
    "v_pk_fma_f32 %2, %2, %13, %9 op_sel:[1,1,0] op_sel_hi:[1,0,1]\n\t"
    "v_pk_fma_f32 %3, %3, %14, %10 op_sel:[1,1,0] op_sel_hi:[1,0,1]\n\t"
    "v_pk_mul_f32 %7, %4, %15 op_sel_hi:[0,1] neg_hi:[1,0]\n\t"
    "v_pk_mul_f32 %8, %5, %16 op_sel_hi:[0,1] neg_hi:[1,0]\n\t"
    "v_pk_mul_f32 %9, %6, %17 op_sel_hi:[0,1] neg_hi:[1,0]\n\t"
    "v_pk_fma_f32 %4, %4, %15, %7 op_sel:[1,1,0] op_sel_hi:[1,0,1]\n\t"
    "v_pk_fma_f32 %5, %5, %16, %8 op_sel:[1,1,0] op_sel_hi:[1,0,1]\n\t"
    "v_pk_fma_f32 %6, %6, %17, %9 op_sel:[1,1,0] op_sel_hi:[1,0,1]\n\t"
    : "+v"(x[0]), "+v"(x[1]), "+v"(x[2]), "+v"(x[3]), "+v"(x[4]), "+v"(x[5]), "+v"(x[6]), "=&v"(t0), "=&v"(t1), "=&v"(t2), "=&v"(t3)
    : "v"(w[0]), "v"(w[1]), "v"(w[2]), "v"(w[3]), "v"(w[4]), "v"(w[5]), "v"(w[6]));
}
__device__ __forceinline__ void tw8_i(f2* x, const f2* w) {
  f2 t0, t1, t2, t3;
  asm(
    "v_pk_mul_f32 %8, %0, %12 op_sel_hi:[0,1] neg_hi:[1,0]\n\t"
    "v_pk_mul_f32 %9, %1, %13 op_sel_hi:[0,1] neg_hi:[1,0]\n\t"
    "v_pk_mul_f32 %10, %2, %14 op_sel_hi:[0,1] neg_hi:[1,0]\n\t"
    "v_pk_mul_f32 %11, %3, %15 op_sel_hi:[0,1] neg_hi:[1,0]\n\t"
    "v_pk_fma_f32 %0, %0, %12, %8 op_sel:[1,1,0] op_sel_hi:[1,0,1]\n\t"
    "v_pk_fma_f32 %1, %1, %13, %9 op_sel:[1,1,0] op_sel_hi:[1,0,1]\n\t"
    "v_pk_fma_f32 %2, %2, %14, %10 op_sel:[1,1,0] op_sel_hi:[1,0,1]\n\t"
    "v_pk_fma_f32 %3, %3, %15, %11 op_sel:[1,1,0] op_sel_hi:[1,0,1]\n\t"
    "v_pk_mul_f32 %8, %4, %16 op_sel_hi:[0,1] neg_hi:[1,0]\n\t"
    "v_pk_mul_f32 %9, %5, %17 op_sel_hi:[0,1] neg_hi:[1,0]\n\t"
    "v_pk_mul_f32 %10, %6, %18 op_sel_hi:[0,1] neg_hi:[1,0]\n\t"
    "v_pk_mul_f32 %11, %7, %19 op_sel_hi:[0,1] neg_hi:[1,0]\n\t"
    "v_pk_fma_f32 %4, %4, %16, %8 op_sel:[1,1,0] op_sel_hi:[1,0,1]\n\t"
    "v_pk_fma_f32 %5, %5, %17, %9 op_sel:[1,1,0] op_sel_hi:[1,0,1]\n\t"
    "v_pk_fma_f32 %6, %6, %18, %10 op_sel:[1,1,0] op_sel_hi:[1,0,1]\n\t"
    "v_pk_fma_f32 %7, %7, %19, %11 op_sel:[1,1,0] op_sel_hi:[1,0,1]\n\t"
    : "+v"(x[0]), "+v"(x[1]), "+v"(x[2]), "+v"(x[3]), "+v"(x[4]), "+v"(x[5]), "+v"(x[6]), "+v"(x[7]), "=&v"(t0), "=&v"(t1), "=&v"(t2), "=&v"(t3)
    : "v"(w[0]), "v"(w[1]), "v"(w[2]), "v"(w[3]), "v"(w[4]), "v"(w[5]), "v"(w[6]), "v"(w[7]));
}
__device__ __forceinline__ void filter4(f2* a, f2* b, const f4* wt, f2 ck) {
  f2 t0, t1, t2, t3, t4, t5, t6, t7;
  asm(
    "v_pk_mul_f32 %8, %0, %24 op_sel_hi:[1,0]\n\t"
    "v_pk_mul_f32 %12, %1, %24 op_sel_hi:[1,0]\n\t"
    "v_pk_mul_f32 %9, %4, %24 op_sel_hi:[1,0]\n\t"
    "v_pk_mul_f32 %13, %5, %24 op_sel_hi:[1,0]\n\t"
    "v_pk_mul_f32 %10, %0, %16 op_sel_hi:[0,1]\n\t"
    "v_pk_mul_f32 %14, %1, %17 op_sel_hi:[0,1]\n\t"
    "v_pk_mul_f32 %11, %4, %16 op_sel_hi:[0,1] neg_hi:[1,0]\n\t"
    "v_pk_mul_f32 %15, %5, %17 op_sel_hi:[0,1] neg_hi:[1,0]\n\t"
    "v_pk_fma_f32 %8, %4, %24, %8 op_sel:[1,1,0] op_sel_hi:[0,1,1] neg_lo:[1,0,0] neg_hi:[1,0,0]\n\t"
    "v_pk_fma_f32 %12, %5, %24, %12 op_sel:[1,1,0] op_sel_hi:[0,1,1] neg_lo:[1,0,0] neg_hi:[1,0,0]\n\t"
    "v_pk_fma_f32 %9, %0, %24, %9 op_sel:[1,1,0] op_sel_hi:[0,1,1]\n\t"
    "v_pk_fma_f32 %13, %1, %24, %13 op_sel:[1,1,0] op_sel_hi:[0,1,1]\n\t"
    "v_pk_fma_f32 %10, %0, %16, %10 op_sel:[1,1,0] op_sel_hi:[1,0,1] neg_lo:[1,0,0]\n\t"
    "v_pk_fma_f32 %14, %1, %17, %14 op_sel:[1,1,0] op_sel_hi:[1,0,1] neg_lo:[1,0,0]\n\t"
    "v_pk_fma_f32 %11, %4, %16, %11 op_sel:[1,1,0] op_sel_hi:[1,0,1]\n\t"
    "v_pk_fma_f32 %15, %5, %17, %15 op_sel:[1,1,0] op_sel_hi:[1,0,1]\n\t"
    "v_pk_fma_f32 %10, %8, %20, %10 op_sel_hi:[0,1,1]\n\t"
    "v_pk_fma_f32 %14, %12, %21, %14 op_sel_hi:[0,1,1]\n\t"
    "v_pk_fma_f32 %11, %9, %20, %11 op_sel_hi:[0,1,1] neg_lo:[1,0,0]\n\t"
    "v_pk_fma_f32 %15, %13, %21, %15 op_sel_hi:[0,1,1] neg_lo:[1,0,0]\n\t"
    "v_pk_fma_f32 %0, %8, %20, %10 op_sel:[1,1,0] op_sel_hi:[1,0,1] neg_lo:[1,0,0]\n\t"
    "v_pk_fma_f32 %1, %12, %21, %14 op_sel:[1,1,0] op_sel_hi:[1,0,1] neg_lo:[1,0,0]\n\t"
    "v_pk_fma_f32 %4, %9, %20, %11 op_sel:[1,1,0] op_sel_hi:[1,0,1] neg_lo:[1,0,0] neg_hi:[1,0,0]\n\t"
    "v_pk_fma_f32 %5, %13, %21, %15 op_sel:[1,1,0] op_sel_hi:[1,0,1] neg_lo:[1,0,0] neg_hi:[1,0,0]\n\t"
    "v_pk_mul_f32 %8, %2, %24 op_sel_hi:[1,0]\n\t"
    "v_pk_mul_f32 %12, %3, %24 op_sel_hi:[1,0]\n\t"
    "v_pk_mul_f32 %9, %6, %24 op_sel_hi:[1,0]\n\t"
    "v_pk_mul_f32 %13, %7, %24 op_sel_hi:[1,0]\n\t"
    "v_pk_mul_f32 %10, %2, %18 op_sel_hi:[0,1]\n\t"
    "v_pk_mul_f32 %14, %3, %19 op_sel_hi:[0,1]\n\t"
    "v_pk_mul_f32 %11, %6, %18 op_sel_hi:[0,1] neg_hi:[1,0]\n\t"
    "v_pk_mul_f32 %15, %7, %19 op_sel_hi:[0,1] neg_hi:[1,0]\n\t"
    "v_pk_fma_f32 %8, %6, %24, %8 op_sel:[1,1,0] op_sel_hi:[0,1,1] neg_lo:[1,0,0] neg_hi:[1,0,0]\n\t"
    "v_pk_fma_f32 %12, %7, %24, %12 op_sel:[1,1,0] op_sel_hi:[0,1,1] neg_lo:[1,0,0] neg_hi:[1,0,0]\n\t"
    "v_pk_fma_f32 %9, %2, %24, %9 op_sel:[1,1,0] op_sel_hi:[0,1,1]\n\t"
    "v_pk_fma_f32 %13, %3, %24, %13 op_sel:[1,1,0] op_sel_hi:[0,1,1]\n\t"
    "v_pk_fma_f32 %10, %2, %18, %10 op_sel:[1,1,0] op_sel_hi:[1,0,1] neg_lo:[1,0,0]\n\t"
    "v_pk_fma_f32 %14, %3, %19, %14 op_sel:[1,1,0] op_sel_hi:[1,0,1] neg_lo:[1,0,0]\n\t"
    "v_pk_fma_f32 %11, %6, %18, %11 op_sel:[1,1,0] op_sel_hi:[1,0,1]\n\t"
    "v_pk_fma_f32 %15, %7, %19, %15 op_sel:[1,1,0] op_sel_hi:[1,0,1]\n\t"
    "v_pk_fma_f32 %10, %8, %22, %10 op_sel_hi:[0,1,1]\n\t"
    "v_pk_fma_f32 %14, %12, %23, %14 op_sel_hi:[0,1,1]\n\t"
    "v_pk_fma_f32 %11, %9, %22, %11 op_sel_hi:[0,1,1] neg_lo:[1,0,0]\n\t"
    "v_pk_fma_f32 %15, %13, %23, %15 op_sel_hi:[0,1,1] neg_lo:[1,0,0]\n\t"
    "v_pk_fma_f32 %2, %8, %22, %10 op_sel:[1,1,0] op_sel_hi:[1,0,1] neg_lo:[1,0,0]\n\t"
    "v_pk_fma_f32 %3, %12, %23, %14 op_sel:[1,1,0] op_sel_hi:[1,0,1] neg_lo:[1,0,0]\n\t"
    "v_pk_fma_f32 %6, %9, %22, %11 op_sel:[1,1,0] op_sel_hi:[1,0,1] neg_lo:[1,0,0] neg_hi:[1,0,0]\n\t"
    "v_pk_fma_f32 %7, %13, %23, %15 op_sel:[1,1,0] op_sel_hi:[1,0,1] neg_lo:[1,0,0] neg_hi:[1,0,0]\n\t"
    : "+v"(a[0]), "+v"(a[1]), "+v"(a[2]), "+v"(a[3]), "+v"(b[0]), "+v"(b[1]), "+v"(b[2]), "+v"(b[3]), "=&v"(t0), "=&v"(t1), "=&v"(t2), "=&v"(t3), "=&v"(t4), "=&v"(t5), "=&v"(t6), "=&v"(t7)
    : "v"(wt[0].xy), "v"(wt[1].xy), "v"(wt[2].xy), "v"(wt[3].xy), "v"(wt[0].zw), "v"(wt[1].zw), "v"(wt[2].zw), "v"(wt[3].zw), "v"(ck));
}
__device__ __forceinline__ void swap4(f2* a, f2* b) {
  float p[8], q[8];
  p[0] = a[0].x; p[1] = a[0].y; q[0] = b[0].x; q[1] = b[0].y;
  p[2] = a[1].x; p[3] = a[1].y; q[2] = b[1].x; q[3] = b[1].y;
  p[4] = a[2].x; p[5] = a[2].y; q[4] = b[2].x; q[5] = b[2].y;
  p[6] = a[3].x; p[7] = a[3].y; q[6] = b[3].x; q[7] = b[3].y;

  asm(
    "s_nop 1\n\t"
    "v_permlane32_swap_b32 %0, %8\n\t"
    "v_permlane32_swap_b32 %1, %9\n\t"
    "v_permlane32_swap_b32 %2, %10\n\t"
    "v_permlane32_swap_b32 %3, %11\n\t"
    "v_permlane32_swap_b32 %4, %12\n\t"
    "v_permlane32_swap_b32 %5, %13\n\t"
    "v_permlane32_swap_b32 %6, %14\n\t"
    "v_permlane32_swap_b32 %7, %15\n\t"
    : "+v"(p[0]), "+v"(p[1]), "+v"(p[2]), "+v"(p[3]), "+v"(p[4]), "+v"(p[5]), "+v"(p[6]), "+v"(p[7]), "+v"(q[0]), "+v"(q[1]), "+v"(q[2]), "+v"(q[3]), "+v"(q[4]), "+v"(q[5]), "+v"(q[6]), "+v"(q[7])
    : );
  a[0] = f2{p[0], p[1]}; b[0] = f2{q[0], q[1]};
  a[1] = f2{p[2], p[3]}; b[1] = f2{q[2], q[3]};
  a[2] = f2{p[4], p[5]}; b[2] = f2{q[4], q[5]};
  a[3] = f2{p[6], p[7]}; b[3] = f2{q[6], q[7]};
}

template <int N> __device__ __forceinline__ void wait_vm() { asm volatile("s_waitcnt vmcnt(%c0)" ::"n"(N) : "memory"); }
template <int N> __device__ __forceinline__ void wait_lgkm() { asm volatile("s_waitcnt lgkmcnt(%c0)" ::"n"(N) : "memory"); }
template <class T> __device__ __forceinline__ void tie(T& v) { asm volatile("" : "+v"(v)); }
template <int OFF> __device__ __forceinline__ void gload1(unsigned& d, unsigned voff, const void* base) {
  asm volatile("global_load_dword %0, %1, %2 offset:%c3" : "=v"(d) : "v"(voff), "s"(base), "n"(OFF) : "memory");
}
template <int OFF, class T> __device__ __forceinline__ void gload2(T& d, unsigned voff, const void* base) {
  asm volatile("global_load_dwordx2 %0, %1, %2 offset:%c3" : "=v"(d) : "v"(voff), "s"(base), "n"(OFF) : "memory");
}
template <int OFF> __device__ __forceinline__ void gload_v(u4& d, unsigned voff, const void* base) {
  asm volatile("global_load_dwordx4 %0, %1, %2 offset:%c3" : "=v"(d) : "v"(voff), "s"(base), "n"(OFF) : "memory");
}
template <int OFF> __device__ __forceinline__ void gload_a(u4& d, unsigned voff, const void* base) {
  asm volatile("global_load_dwordx4 %0, %1, %2 offset:%c3" : "=a"(d) : "v"(voff), "s"(base), "n"(OFF) : "memory");
}
#define ST_POL ""
template <int OFF> __device__ __forceinline__ void gstore1(unsigned voff, unsigned d, void* base) {
  asm volatile("global_store_dword %0, %1, %2 offset:%c3" ST_POL ::"v"(voff), "v"(d), "s"(base), "n"(OFF) : "memory");
}
template <int OFF, class T> __device__ __forceinline__ void gstore2(unsigned voff, T d, void* base) {
  asm volatile("global_store_dwordx2 %0, %1, %2 offset:%c3" ST_POL ::"v"(voff), "v"(d), "s"(base), "n"(OFF) : "memory");
}
template <int OFF> __device__ __forceinline__ void gstore4(unsigned voff, u4 d, void* base) {
  asm volatile("global_store_dwordx4 %0, %1, %2 offset:%c3" ST_POL "\n\ts_nop 1" ::"v"(voff), "v"(d), "s"(base), "n"(OFF) : "memory");
}
template <int N> __device__ __forceinline__ void code_touch(unsigned (&d)[N], int lane) {
  unsigned long long pc;
  int oe;
  asm volatile("s_getpc_b64 %0\n\ts_mov_b32 %1, code_end_mark@rel32@lo+4" : "=s"(pc), "=s"(oe));
  const int lim = oe > 4 ? oe - 4 : 0;
#pragma unroll
  for (int i = 0; i < N; ++i) {
    int o = lane * 128 + i * 8192;
    gload1<0>(d[i], (unsigned)(o < lim ? o : lim) & ~3u, reinterpret_cast<const void*>(pc));
  }
}
__device__ __forceinline__ f2 cis(float x) { return f2{__builtin_amdgcn_cosf(x), __builtin_amdgcn_sinf(-x)}; }
template <int I, int N, class F> __device__ __forceinline__ void static_for(F&& f) {
  if constexpr (I < N) { f(std::integral_constant<int, I>{}); static_for<I + 1, N>(f); }
}

__device__ __forceinline__ unsigned fbits(float v) { return __builtin_bit_cast(unsigned, v); }
__device__ __forceinline__ float bitsf(unsigned v) { return __builtin_bit_cast(float, v); }
__device__ __forceinline__ unsigned pack_bf16(float lo, float hi) {
  f2 v = {lo, hi};
  bf2v b = __builtin_convertvector(v, bf2v);
  return __builtin_bit_cast(unsigned, b);
}
__device__ __forceinline__ float bflo(unsigned u) { return bitsf(u << 16); }
__device__ __forceinline__ float bfhi(unsigned u) { return bitsf(u & 0xffff0000u); }
__device__ __forceinline__ f2 unpack_bf16(unsigned u) { return f2{bflo(u), bfhi(u)}; }
__device__ __forceinline__ f2 bf16r2(f2 v) { return unpack_bf16(pack_bf16(v.x, v.y)); }
__device__ __forceinline__ float bf16r(float v) { return bitsf(pack_bf16(v, v) << 16); }
__device__ __forceinline__ float bf2f(unsigned short b) { return bitsf((unsigned)b << 16); }
template <int CTRL, int RMASK> __device__ __forceinline__ float dpp_add(float v) {
  int r = __builtin_amdgcn_update_dpp(0, __builtin_bit_cast(int, v), CTRL, RMASK, 0xf, false);
  return v + __builtin_bit_cast(float, r);
}
__device__ __forceinline__ float wave_sum(float v) {
  v = dpp_add<0xB1, 0xf>(v);
  v = dpp_add<0x4E, 0xf>(v);
  v = dpp_add<0x141, 0xf>(v);
  v = dpp_add<0x140, 0xf>(v);
  v = dpp_add<0x142, 0xa>(v);
  v = dpp_add<0x143, 0xc>(v);
  return __builtin_bit_cast(float, __builtin_amdgcn_readlane(__builtin_bit_cast(int, v), 63));
}
__device__ __forceinline__ void layernorm_row(f2 (&z)[5]) {
  float z00 = z[0].x;
  float piv = bitsf(__builtin_amdgcn_readfirstlane(fbits(z00)));
  f2 s1 = {0.f, 0.f}, s2 = {0.f, 0.f};
#pragma unroll
  for (int i = 0; i < 5; ++i) { f2 d = z[i] - piv; s1 += d; s2 += d * d; }
  float a = wave_sum(s1.x + s1.y) * (1.f / 640.f), b = wave_sum(s2.x + s2.y) * (1.f / 640.f);
  float mean = bf16r(piv + a);
  float var = bf16r(fmaxf(b - a * a, 0.f));
  float den = bf16r(__builtin_sqrtf(bf16r(var + 1e-5f)));
  float rden = 1.f / den;
#pragma unroll
  for (int i = 0; i < 5; ++i) z[i] = bf16r2(z[i] - mean) * rden;
}

__device__ __forceinline__ int elem_off(int p, int r) {
  int reg = ((p >> 4) * 8 + r) * 16 + (p & 15);
  int s0 = r <= 4 ? r * 16 : (8 - r) * 16 + 8;
  int s8 = r == 0 ? 160 * 16 + 8 : (r == 4 ? 160 * 16 + 9 : (r < 4 ? (8 - r) * 16 + 8 : r * 16));
  return p == 0 ? s0 : (p == 8 ? s8 : reg);
}
__device__ __forceinline__ int col_of(int p) {
  int kk = (p >> 4) * 8 + (p & 7);
  return (p & 8) ? (kk == 0 ? 160 : 320 - kk) : kk;
}
#define HST_POL " sc1"
__device__ __forceinline__ void HST(u4* p, u4 v) { asm volatile("global_store_dwordx4 %0, %1, off" HST_POL "\n\ts_nop 1" ::"v"(p), "v"(v) : "memory"); }
__device__ __forceinline__ void act_clear(u4* __restrict__ ACT, int frag, int lane) {
  asm volatile("global_store_dwordx4 %0, %1, %2 sc1\n\ts_nop 1" ::"v"((frag * 64 + lane) * 16), "v"(u4{~0u, ~0u, ~0u, ~0u}), "s"(ACT) : "memory");
}
__device__ __forceinline__ void tr8x2(const unsigned (&r)[8], u4& lo, u4& hi) {
#pragma unroll
  for (int i = 0; i < 4; ++i) {
    lo[i] = __builtin_amdgcn_perm(r[2 * i + 1], r[2 * i], 0x05040100u);
    hi[i] = __builtin_amdgcn_perm(r[2 * i + 1], r[2 * i], 0x07060302u);
  }
}
struct W1Regs { u2 a[8], b[8]; };
__device__ __forceinline__ void w1_load(W1Regs& R, int wi, int lane, const u2* __restrict__ wg, const u2* __restrict__ wu) {
  const int kap = wi / 24, Fq = (wi - kap * 24) * 16 + (lane & 15), kq = kap * 4 + (lane >> 4);
  const unsigned vo = (kq * 8 * 384 + Fq) * 8;
  static_for<0, 8>([&](auto I) {
    gload2<(I & 1) * 3072>(R.a[I], vo, wg + (I >> 1) * 768);
    gload2<(I & 1) * 3072>(R.b[I], vo, wu + (I >> 1) * 768);
  });
}
__device__ __forceinline__ void w1_store(W1Regs& R, int wi, int lane, u4* __restrict__ st, u4* __restrict__ W1F) {
#pragma unroll
  for (int i = 0; i < 8; ++i) { tie(R.a[i]); tie(R.b[i]); }
  const int kap = wi / 24, Fg = wi - kap * 24, fl = lane & 15, q = lane >> 4;
  u4* wp = st + (fl >> 1) * 64 + q * 16 + (fl & 1) * 8;
#pragma unroll
  for (int h = 0; h < 2; ++h) {
    unsigned ra[8], rb[8];
#pragma unroll
    for (int i = 0; i < 8; ++i) { ra[i] = h ? R.a[i].y : R.a[i].x; rb[i] = h ? R.b[i].y : R.b[i].x; }
    u4 g0, g1, u0, u1;
    tr8x2(ra, g0, g1); tr8x2(rb, u0, u1);
    wp[(4 * h + 0) ^ (fl & 7)] = g0; wp[(4 * h + 1) ^ (fl & 7)] = g1; wp[(4 * h + 2) ^ (fl & 7)] = u0; wp[(4 * h + 3) ^ (fl & 7)] = u1;
  }
#pragma unroll
  for (int tl = 0; tl < 8; ++tl) {
    const int T = Fg * 8 + tl, cw = T / 3, t = T - cw * 3;
    HST(W1F + ((cw * 20 + kap) * 3 + t) * 64 + lane, st[tl * 64 + (lane ^ ((tl * 2 + ((lane >> 3) & 1)) & 7))]);
  }
}
__device__ __forceinline__ void w2_load(W1Regs& R, int wi, int lane, const u2* __restrict__ wd) {
  const int kap = wi / 10, cq = (wi - kap * 10) * 16 + (lane & 15), q = lane >> 4, cb = kap / 3, t = kap - cb * 3;
  static_for<0, 8>([&](auto I) { gload2<(I & 1) * 1280>(R.a[I], ((cb * 96 + ((I >> 1) ^ q) * 24 + t * 8 + 2 * q) * 160 + cq) * 8, wd); });
}
__device__ __forceinline__ void w2_store(W1Regs& R, int wi, int lane, u4* __restrict__ st, u4* __restrict__ W2F) {
#pragma unroll
  for (int i = 0; i < 8; ++i) tie(R.a[i]);
  const int kap = wi / 10, cg = wi - kap * 10, cl = lane & 15, q = lane >> 4;
  u4* wp = st + (cl >> 2) * 64 + q * 16 + (cl & 3) * 4;
  const int sw = (cl >> 1) & 3;
#pragma unroll
  for (int h = 0; h < 2; ++h) {
    unsigned ra[8];
#pragma unroll
    for (int i = 0; i < 8; ++i) ra[i] = h ? R.a[i].y : R.a[i].x;
    u4 c0, c1;
    tr8x2(ra, c0, c1);
    wp[(2 * h) ^ sw] = c0; wp[(2 * h + 1) ^ sw] = c1;
  }
#pragma unroll
  for (int tl = 0; tl < 4; ++tl)
    HST(W2F + ((cg * 4 + tl) * 48 + kap) * 64 + lane, st[tl * 64 + (lane ^ ((tl * 2 + ((lane >> 3) & 1)) & 3))]);
}
typedef unsigned u4d __attribute__((ext_vector_type(4), aligned(4)));
template <int SH> __device__ __forceinline__ void ld8(unsigned (&d)[5], const unsigned short* __restrict__ w, int o) {
  const unsigned* p = reinterpret_cast<const unsigned*>(w + (o - SH));
  u4 v = *reinterpret_cast<const u4d*>(p);
  d[0] = v[0]; d[1] = v[1]; d[2] = v[2]; d[3] = v[3];
  if constexpr (SH) d[4] = p[4];
}
#define BAR() asm volatile("s_waitcnt lgkmcnt(0)\n\ts_barrier" ::: "memory")
template <int ODD> __device__ __forceinline__ void wt_body(int r, int g, int cl, int side, int i, const unsigned short* __restrict__ wr,
                                                           const unsigned short* __restrict__ wi, u4* __restrict__ WT) {
  unsigned ar[2][5], ai[2][5], br[2][5], bi[2][5];
#pragma unroll
  for (int h = 0; h < 2; ++h) {
    int ks = 8 * (cl + 16 * (2 * i + h) + 128 * side) + r, mks = (2048 - ks) & 2047;
    int oa = ks * 321 + 8 * g, ob = mks * 321 + 313 - 8 * g;
    ld8<ODD>(ar[h], wr, oa); ld8<ODD>(ai[h], wi, oa);
    ld8<1 - ODD>(br[h], wr, ob); ld8<1 - ODD>(bi[h], wi, ob);
  }
  BAR(); BAR(); BAR();
  u4* dst = WT + ((8 * g + r) * 4 + i) * 256 + side * 32 + cl;
#pragma unroll
  for (int p = 0; p < 8; ++p) {
    const int ea = p + ODD, eb = 7 - p + 1 - ODD;
    const unsigned sa = (ea & 1) ? 0x07060302u : 0x05040100u, sb = (eb & 1) ? 0x07060302u : 0x05040100u;
    u4 o;
#pragma unroll
    for (int h = 0; h < 2; ++h) {
      o[2 * h] = __builtin_amdgcn_perm(ai[h][ea >> 1], ar[h][ea >> 1], sa);
      o[2 * h + 1] = __builtin_amdgcn_perm(bi[h][eb >> 1], br[h][eb >> 1], sb);
    }
    if (p > 0 || g > 0 || r < 5) HST(dst + (p >> 1) * 64 + (p & 1) * 16, o);
  }
}
__device__ __forceinline__ void wt_item(int wid, int lane, const unsigned short* __restrict__ wr, const unsigned short* __restrict__ wi,
                                        u4* __restrict__ WT) {
  const int r = wid & 7, rest = wid >> 3, si = rest / 5, g4 = rest - si * 5, side = si >> 2, i = si & 3;
  const int cl = lane & 15, g = g4 * 4 + (lane >> 4);
  if (r & 1) wt_body<1>(r, g, cl, side, i, wr, wi, WT);
  else wt_body<0>(r, g, cl, side, i, wr, wi, WT);
}
__device__ __forceinline__ void ws_item(int it, const unsigned short* __restrict__ wr, const unsigned short* __restrict__ wi,
                                        u2* __restrict__ WT2) {
  const int row = it >> 8, q = it & 255;
  const int roff = row < 4 ? 3 - row : (row == 4 ? 4 : row - 5);
  const int ks = 8 * q + roff, mks = (2048 - ks) & 2047;
  const int oa = ks * 321 + (row < 5 ? 160 : 320), ob = mks * 321 + (row < 5 ? 160 : 0);
  const unsigned e0 = wr[oa], e1 = wi[oa], e2 = wr[ob], e3 = wi[ob];
  BAR(); BAR(); BAR();
  u2 o = {e0 | (e1 << 16), e2 | (e3 << 16)};
  const int cl = q & 15, dd = (q >> 4) & 7, side = q >> 7, i = dd >> 1, h = dd & 1;
  int idx;
  if (row < 5) {
    int u = row < 3 ? 5 + row : 160, p = row == 4;
    idx = ((u * 4 + i) * 256 + side * 32 + p * 16 + cl) * 2 + h;
  } else {
    idx = 161 * 2048 + ((((row - 5) * 4 + i) * 2 + side) * 16 + cl) * 2 + h;
  }
  WT2[idx] = o;
}
__device__ __forceinline__ void code_prefetch(int lane) {
  unsigned long long pc;
  int ob, oe;
  asm volatile("s_getpc_b64 %0\n\ts_mov_b32 %1, k2_kernel@rel32@lo+4\n\ts_mov_b32 %2, code_end_mark@rel32@lo+12" : "=s"(pc), "=s"(ob), "=s"(oe));
  const void* b = reinterpret_cast<const void*>(pc + (long long)ob);
  const int lim = oe - ob > 4 ? oe - ob - 4 : 0;
  unsigned d[6];
#pragma unroll
  for (int i = 0; i < 6; ++i) {
    int o = lane * 128 + i * 8192;
    gload1<0>(d[i], (unsigned)(o < lim ? o : lim) & ~3u, b);
  }
  BAR(); BAR(); BAR();
  wait_vm<0>();
#pragma unroll
  for (int i = 0; i < 6; ++i) tie(d[i]);
}

template <int OFF, class T> __device__ __forceinline__ void gload2c(T& d, unsigned voff, const void* base) {
  asm volatile("global_load_dwordx2 %0, %1, %2 offset:%c3 sc1" : "=v"(d) : "v"(voff), "s"(base), "n"(OFF) : "memory");
}
template <int OFF, class T> __device__ __forceinline__ void gstore2c(unsigned voff, T d, void* base) {
  asm volatile("global_store_dwordx2 %0, %1, %2 offset:%c3 sc1" ::"v"(voff), "v"(d), "s"(base), "n"(OFF) : "memory");
}
template <int OFF> __device__ __forceinline__ void gstore4c(unsigned voff, u4 d, void* base) {
  asm volatile("global_store_dwordx4 %0, %1, %2 offset:%c3 sc1\n\ts_nop 1" ::"v"(voff), "v"(d), "s"(base), "n"(OFF) : "memory");
}
#define G1ST4 gstore4
#define G1ST1 gstore1
typedef _Float16 h2v __attribute__((ext_vector_type(2)));
__device__ __forceinline__ unsigned pack_h(f2 v) { return __builtin_bit_cast(unsigned, __builtin_convertvector(v, h2v)); }
__device__ __forceinline__ f2 unpack_h(unsigned u) { return __builtin_convertvector(__builtin_bit_cast(h2v, u), f2); }
__device__ __forceinline__ u4 quad_tr(unsigned a0, unsigned a1, unsigned a2, unsigned a3, bool b0, bool b1) {
  const unsigned k01 = b0 ? a1 : a0, k23 = b0 ? a3 : a2;
  const unsigned r01 = __builtin_amdgcn_mov_dpp(b0 ? a0 : a1, 0xB1, 0xf, 0xf, true), r23 = __builtin_amdgcn_mov_dpp(b0 ? a2 : a3, 0xB1, 0xf, 0xf, true);
  return u4{b1 ? k23 : k01, b1 ? r23 : r01, (unsigned)__builtin_amdgcn_mov_dpp(b1 ? k01 : k23, 0x4E, 0xf, 0xf, true),
            (unsigned)__builtin_amdgcn_mov_dpp(b1 ? r01 : r23, 0x4E, 0xf, 0xf, true)};
}
template <int OFF> __device__ __forceinline__ void gload1s(unsigned& d, unsigned voff, const void* base) {
  asm volatile("global_load_dword %0, %1, %2 offset:%c3 sc1" : "=v"(d) : "v"(voff), "s"(base), "n"(OFF) : "memory");
}
template <int OFF> __device__ __forceinline__ void gstore1c(unsigned voff, unsigned d, void* base) {
  asm volatile("global_store_dword %0, %1, %2 offset:%c3 sc1" ::"v"(voff), "v"(d), "s"(base), "n"(OFF) : "memory");
}
__device__ __forceinline__ int row_of(int b) { return (b & ~15) | ((b & 7) << 1) | ((b >> 3) & 1); }
constexpr int E_RS = 72;
constexpr int E3_RS = 328;
extern "C" __global__ __launch_bounds__(512) void k1_kernel(const unsigned* __restrict__ x, unsigned* __restrict__ G1, unsigned* __restrict__ SC,
                                                 const unsigned short* __restrict__ wr, const unsigned short* __restrict__ wi,
                                                 u4* __restrict__ WT, u4* __restrict__ ACT DBG_ARG) {
  __shared__ __attribute__((aligned(16))) f2 lds[2 * 40 * E_RS];
  unsigned ct[1] = {0};
  if (threadIdx.x >= 448) code_touch<1>(ct, threadIdx.x & 63);
  STAMP_DECL
  STAMP(0);
  f2* E1 = lds; f2* E2 = lds + 40 * E_RS; f2* E3 = lds;
  const int s0 = row_of(blockIdx.x), tid = threadIdx.x, lane = tid & 63, j = tid >> 6;
  const bool act = tid < 320;
  const int rho = tid >> 3, lo = tid & 7;
  f2 tw64[7], tws[8];
  unsigned so[8];
  {
    unsigned raw[5];
    f2 tw[4];
    static_for<0, 5>([&](auto I) { gload1<I * 256>(raw[I], ((s0 + 256 * j) * 320 + lane) * 4, x); });
    act_clear(ACT, s0 * 8 + j, lane);
    const float lf = (float)lane;
#pragma unroll
    for (int k = 0; k < 4; ++k) { tw[k] = cis(lf * ((k + 1) / 320.f)); tie(tw[k]); }
    if (act) {
      const float lof = (float)lo, b = (float)s0 * (1.f / 2048.f);
      const float off = (tid & 8) ? 8.f * b : 0.f, step = (tid & 8) ? -b : b;
#pragma unroll
      for (int c = 0; c < 7; ++c) { tw64[c] = cis(lof * ((c + 1) / 64.f)); tie(tw64[c]); }
#pragma unroll
      for (int r = 0; r < 8; ++r) {
        tws[r] = cis(__builtin_fmaf((float)r, step, off)) * 8.f; tie(tws[r]);
        int o = elem_off(tid, r);
        so[r] = (((o >> 4) * 256 + s0) * 16 + ((o & 15) ^ ((o >> 4) & 3))) * 4; tie(so[r]);
      }
    }
    wait_vm<1>();
    tie(ct[0]);
    f2 z[5];
#pragma unroll
    for (int i = 0; i < 5; ++i) { tie(raw[i]); z[i] = unpack_bf16(raw[i]); }
    STAMP(1);
    layernorm_row(z);
#pragma unroll
    for (int i = 0; i < 5; ++i) z[i] = bf16r2(z[i]);
    fft5_f(z);
    tw4_f(z + 1, tw);
#pragma unroll
    for (int k = 0; k < 5; ++k) E1[(j * 5 + k) * E_RS + lane] = z[k];
  }
  if (!act) {
    const int hs = __builtin_amdgcn_readfirstlane((j - 5) * 256 + blockIdx.x);
    if (hs < 320) wt_item(hs, lane, wr, wi, WT);
    else if (hs < 360) ws_item((hs - 320) * 64 + lane, wr, wi, reinterpret_cast<u2*>(WT));
    else if (hs < 368) code_prefetch(lane);
    else BAR();
    return;
  }
  BAR();
  STAMP(2);
  {
    f2 v[8];
#pragma unroll
    for (int l1 = 0; l1 < 8; ++l1) v[l1] = E1[rho * E_RS + lo + 8 * l1];
    fft8_f(v);
    tw7_f(v + 1, tw64);
#pragma unroll
    for (int c = 0; c < 8; ++c) E2[rho * E_RS + c * 9 + lo] = v[c];
  }
  BAR();
  STAMP(3);
  {
    f2 v[8];
#pragma unroll
    for (int l0 = 0; l0 < 8; ++l0) v[l0] = E2[rho * E_RS + lo * 9 + l0];
    fft8_f(v);
    int jj = rho / 5, k1 = rho - jj * 5;
#pragma unroll
    for (int c0 = 0; c0 < 8; ++c0) E3[jj * E3_RS + k1 + 5 * lo + 40 * c0] = v[c0];
  }
  BAR();
  STAMP(4);
  {
    const int k = col_of(tid), side = (tid >> 3) & 1;
    f2 v[8];
#pragma unroll
    for (int jj = 0; jj < 8; ++jj) v[jj] = E3[(side ? ((8 - jj) & 7) : jj) * E3_RS + k];
    fft8_f(v);
    f2 t0 = v[0] * 8.f;
    tw8_f(v, tws);
    unsigned w[8];
#pragma unroll
    for (int r = 0; r < 8; ++r) w[r] = pack_h(v[r]) | 1u;
    const u4 o0 = quad_tr(w[0], w[1], w[2], w[3], tid & 1, tid & 2), o1 = quad_tr(w[4], w[5], w[6], w[7], tid & 1, tid & 2);
    if (tid >= 16) {
      const unsigned vo4 = ((((tid >> 4) * 8 + (tid & 3)) * 256 + s0) * 16 + (tid & 12)) * 4;
      G1ST4<0>(vo4, o0, G1);
      G1ST4<0>(vo4, o1, G1 + 16384);
    } else {
#pragma unroll
      for (int r = 0; r < 8; ++r) G1ST1<0>(so[r], w[r], G1);
      if (tid == 0) {
        G1ST1<0>(((4 * 256 + s0) * 16 + 8) * 4, w[4], G1);
        G1ST1<0>(((0 * 256 + s0) * 16 + 8) * 4, pack_h(cmul(t0, cis((float)s0 * (1.f / 256.f)))) | 1u, G1);
        G1ST1<0>(s0 * 4, 1u, SC);
      }
      if (tid == 8) {
        G1ST1<0>(((160 * 256 + s0) * 16 + 1) * 4, w[4], G1);
        G1ST1<0>(((160 * 256 + s0) * 16 + 0) * 4, pack_h(t0) | 1u, G1);
        G1ST1<0>(((160 * 256 + s0) * 16 + 10) * 4, 1u, G1);
        G1ST1<0>(((160 * 256 + s0) * 16 + 11) * 4, 1u, G1);
      }
    }
  }
  STAMP(5);
  STAMP_OUT(dbg, 0);
}

constexpr int X_FS = 273;
__device__ __forceinline__ f4 sd_of(unsigned A, unsigned B) {
  f2 wa = unpack_bf16(A), wb = unpack_bf16(B);
  f2 S = pk_add<0, 0, 1>(wa, wb), Dd = pk_add<0, 1, 0>(wa, wb);
  return f4{S.x, S.y, Dd.x, Dd.y};
}
template <int CTRL, int RMASK> __device__ __forceinline__ unsigned dpp_max(unsigned v) {
  return max(v, (unsigned)__builtin_amdgcn_update_dpp(0, (int)v, CTRL, RMASK, 0xf, false));
}
__device__ __forceinline__ unsigned wave_max(unsigned v) {
  v = dpp_max<0xB1, 0xf>(v); v = dpp_max<0x4E, 0xf>(v); v = dpp_max<0x141, 0xf>(v); v = dpp_max<0x140, 0xf>(v);
  v = dpp_max<0x142, 0xa>(v); v = dpp_max<0x143, 0xc>(v);
  return __builtin_amdgcn_readlane(v, 63);
}
#define SPIN_CAP2 2048
#define CLR_SLEEP 48
#define GATE_SLEEP 4
extern "C" __global__ __launch_bounds__(512) void k2_kernel(unsigned* __restrict__ G, unsigned* __restrict__ SC, const u4* __restrict__ WT, const unsigned* __restrict__ x,
                                                 unsigned* __restrict__ Hout, unsigned* __restrict__ AF,
                                                 const u2* __restrict__ wg, const u2* __restrict__ wu, u4* __restrict__ W1F,
                                                 const u2* __restrict__ wd, u4* __restrict__ W2F, u4* __restrict__ ACT DBG_ARG) {
  __shared__ __attribute__((aligned(16))) f2 lds[2 * 40 * E_RS];
  __shared__ __attribute__((aligned(16))) f2 X[16 * X_FS];
  __shared__ __attribute__((aligned(16))) u4 hst[3 * 512];
  __shared__ __attribute__((aligned(16))) unsigned stat[16];
  STAMP_DECL
  STAMP(0);
  f2* E1 = lds; f2* E2 = lds + 40 * E_RS; f2* E3 = lds;
  const int bid = blockIdx.x, s0 = row_of(bid), tid = threadIdx.x, lane = tid & 63, j = __builtin_amdgcn_readfirstlane(tid >> 6);
  const int s = s0 + 256 * j;
  unsigned raw[5];
  f2 tw4[4];
  auto unit = [&](int u) __attribute__((always_inline)) {
    const int wv = j;
    char* gb = reinterpret_cast<char*>(G) + (size_t)u * 16384 + 4096;
    u4 wq[4], ws[4];
    f2 v[16], tw[16], ck;
    unsigned g[16];
    static_for<0, 16>([&](auto I) { gload1s<(I & 7) * 1024 - 4096>(g[I], (tid ^ (u & 3)) * 4, gb + (I >> 3) * 8192); });
    static_for<0, 4>([&](auto I) {
      gload_v<(I & 1) * 4096 - 4096>(wq[I], tid * 16, reinterpret_cast<const char*>(WT) + (size_t)u * 16384 + (I >> 1) * 8192 + 4096);
    });
    int ln = lane;
    tie(ln);
    const int f = ln & 15, t = 4 * wv + (ln >> 4);
    const int side = ln >> 5, pl = (ln >> 4) & 1, cl = ln & 15;
    const int fi = side * 8 + 2 * wv + pl, ci_ = side ? 15 - cl : cl;
    const bool special = u <= 4 && wv == 0;
    if (__builtin_expect(special, 0))
      static_for<0, 4>([&](auto I) {
        gload_v<I * 512>(ws[I], (side * 16 + cl) * 16, reinterpret_cast<const char*>(WT) + (size_t)161 * 16384 + u * 2048);
      });
    act_clear(ACT, 2048 + s0 * 16 + j * 2, ln);
    act_clear(ACT, 2048 + s0 * 16 + j * 2 + 1, ln);
    {
      const float tb = (float)t * (1.f / 256.f), off = (f & 8) ? -16.f * tb : 0.f;
#pragma unroll
      for (int c = 0; c < 16; ++c) { tw[c] = cis(__builtin_fmaf((float)c, tb, off)); tie(tw[c]); }
      const int kk = (u >> 3) * 8 + 2 * wv + pl;
      const int kA = u >= 160 ? 160 : (kk > 0 ? kk : (u <= 4 ? 0 : 160));
      const float a = (float)kA * (1.f / 640.f);
      ck = f2{__builtin_amdgcn_sinf(-a), -__builtin_amdgcn_cosf(a)};
      tie(ck);
    }
    if (special) wait_vm<10>(); else wait_vm<6>();
#pragma unroll
    for (int i = 0; i < 16; ++i) tie(g[i]);
    STAMP(1);
    {
      unsigned m = ~0u;
#pragma unroll
      for (int i = 0; i < 16; ++i) { m = m & g[i]; v[i] = unpack_h(g[i]); }
      const bool unused = u == 160 && (f & 6) != 0;
      stat[wv] = __builtin_amdgcn_ballot_w64((m & 1u) == 0 && !unused) != 0;
    }
    fft16_f(v);
    tw8_f(v, tw); tw8_f(v + 8, tw + 8);
    f2* xo = X + f * X_FS + ((f & 8) ? ((16 - t) & 15) : t) * 16;
    f2* xi = X + fi * X_FS + ci_;
#pragma unroll
    for (int c = 0; c < 16; ++c) xo[c] = v[c];
    wait_vm<2>();
    f4 wt[8];
#pragma unroll
    for (int i = 0; i < 4; ++i) {
      tie(wq[i]);
      wt[2 * i] = sd_of(wq[i][0], wq[i][1]);
      wt[2 * i + 1] = sd_of(wq[i][2], wq[i][3]);
    }
    if (__builtin_expect(special, 0)) {
#pragma unroll
      for (int i = 0; i < 4; ++i) {
        tie(ws[i]);
#pragma unroll
        for (int h = 0; h < 2; ++h) {
          f4 e = sd_of(ws[i][2 * h], ws[i][2 * h + 1]), w = wt[2 * i + h];
          f4 m = {0.5f * (w[0] + e[0]), 0.5f * (w[1] + e[1]), 0.5f * (w[2] - e[2]), 0.5f * (w[3] - e[3])};
          if (pl == 0) wt[2 * i + h] = m;
        }
      }
    }
#pragma unroll
    for (int i = 0; i < 8; ++i) tie(wt[i]);
    BAR();
#pragma unroll
    for (int tt = 0; tt < 16; ++tt) v[tt] = xi[tt * 16];
    const u4 sv = *reinterpret_cast<const u4*>(stat);
    fft16_f(v);
    swap4(v, v + 8); swap4(v + 4, v + 12);
    filter4(v, v + 8, wt, ck); filter4(v + 4, v + 12, wt + 4, ck);
    swap4(v, v + 8); swap4(v + 4, v + 12);
    fft16_i(v);
#pragma unroll
    for (int tt = 0; tt < 16; ++tt) xi[tt * 16] = v[tt];
    const bool taken = __builtin_amdgcn_readfirstlane(sv[0] | sv[1] | sv[2] | sv[3]) != 0;
    {
      float m = 0.f;
#pragma unroll
      for (int i = 0; i < 16; ++i) m = __builtin_fmaxf(__builtin_fmaxf(m, __builtin_fabsf(v[i].x)), __builtin_fabsf(v[i].y));
      stat[12 + wv] = (u == 160 && wv != 0) ? 0u : wave_max(fbits(m));
    }
    BAR();
#pragma unroll
    for (int c = 0; c < 16; ++c) v[c] = xo[c];
    const u4 mv = *reinterpret_cast<const u4*>(stat + 12);
    tw8_i(v, tw); tw8_i(v + 8, tw + 8);
    fft16_i(v);
    const unsigned eb = max((unsigned)__builtin_amdgcn_readfirstlane(max(max(mv[0], mv[1]), max(mv[2], mv[3]))) >> 23, 64u);
    const float sc = bitsf((264u - eb) << 23);
    if (__builtin_expect(!taken, 1)) {
      if (tid == 0) gstore1c<0>(u * 4, (eb - 10u) << 23, SC);
      const unsigned vo = (tid & ~3) * 4 + (f & 3) * 1024;
      static_for<0, 4>([&](auto I) {
        unsigned w[4];
#pragma unroll
        for (int q = 0; q < 4; ++q) w[q] = pack_h(v[4 * I + q] * sc) & ~1u;
        gstore4c<(I & 1) * 4096 - 4096>(vo, quad_tr(w[0], w[1], w[2], w[3], f & 1, f & 2), gb + (I >> 1) * 8192);
      });
    }
  };
  if (__builtin_expect(bid >= 161, 0)) {
    const int wi = (7 - j) * 95 + bid - 161;
    W1Regs R;
    if (wi < 480) {
      w1_load(R, wi, lane, wg, wu);
      wait_vm<0>();
      w1_store(R, wi, lane, j >= 5 ? hst + (j - 5) * 512 : reinterpret_cast<u4*>(X) + (j - 1) * 512, W1F);
    } else __builtin_amdgcn_s_sleep(CLR_SLEEP);
    act_clear(ACT, 2048 + s0 * 16 + j * 2, lane);
    act_clear(ACT, 2048 + s0 * 16 + j * 2 + 1, lane);
  }
  if (__builtin_expect(j < 5, 1)) {
    f2 tw64[7];
    int rho, lo;
    if (__builtin_expect(bid < 161, 1)) {
      if (__builtin_expect(j < 4, 1)) unit(bid);
      else {
        BAR();
        act_clear(ACT, 2048 + s0 * 16 + j * 2, lane);
        act_clear(ACT, 2048 + s0 * 16 + j * 2 + 1, lane);
        BAR();
      }
    }
    for (;;) {
      STAMP(2);
      int td = tid;
      tie(td);
      const int k = col_of(td), side = (td >> 3) & 1;
      rho = td >> 3; lo = td & 7;
      f2 v[8], tws[8];
      unsigned so[8], uo[8], g[8], sc[8];
      static_for<0, 5>([&](auto I) { gload1<I * 256>(raw[I], (s * 320 + (td & 63)) * 4, x); });
      const float lof = (float)lo, b = (float)s0 * (1.f / 2048.f);
      const float off = side ? 8.f * b : 0.f, step = side ? -b : b;
#pragma unroll
      for (int r = 0; r < 8; ++r) {
        int o = elem_off(td, r);
        so[r] = (((o >> 4) * 256 + s0) * 16 + ((o & 15) ^ ((s0 >> 4) & 3))) * 4; tie(so[r]);
        uo[r] = (o >> 4) * 4; tie(uo[r]);
      }
#pragma unroll
      for (int r = 0; r < 8; ++r) { tws[r] = cis(__builtin_fmaf((float)r, step, off)) * (0.0625f / (320.f * 2048.f)); tie(tws[r]); }
#pragma unroll
      for (int c = 0; c < 7; ++c) { tw64[c] = cis(lof * ((c + 1) / 64.f)); tie(tw64[c]); }
      int spins = 0;
      if (__builtin_expect(bid >= 161 || j == 4, 0)) {
#pragma unroll 1
        for (; spins < SPIN_CAP2; ++spins) {
          gload1s<0>(g[0], so[7], G);
          wait_vm<0>();
          tie(g[0]);
          if (__builtin_amdgcn_ballot_w64((g[0] & 1u) == 0) != 0) break;
          __builtin_amdgcn_s_sleep(GATE_SLEEP);
        }
      }
      bool late;
      for (;; ++spins) {
        static_for<0, 8>([&](auto R) { gload1s<0>(g[R], so[R], G); });
        static_for<0, 8>([&](auto R) { gload1s<0>(sc[R], uo[R], SC); });
        wait_vm<0>();
        unsigned m = 0;
#pragma unroll
        for (int r = 0; r < 8; ++r) { tie(g[r]); tie(sc[r]); m = m | g[r] | sc[r]; }
        late = __builtin_amdgcn_ballot_w64(m & 1u) != 0;
        if (!late || spins >= SPIN_CAP2) break;
      }
#pragma unroll
      for (int r = 0; r < 8; ++r) v[r] = unpack_h(g[r]) * bitsf(sc[r]);
      STAMP(3);
      stat[4 + j] = late;
      tw8_i(v, tws);
      fft8_i(v);
#pragma unroll
      for (int jj = 0; jj < 8; ++jj) E3[(side ? ((8 - jj) & 7) : jj) * E3_RS + k] = v[jj];
      BAR();
      const u4 sv = *reinterpret_cast<const u4*>(stat + 4);
      const unsigned s4 = stat[8];
      {
        int jj = rho / 5, k1 = rho - jj * 5;
#pragma unroll
        for (int c0 = 0; c0 < 8; ++c0) v[c0] = E3[jj * E3_RS + k1 + 5 * lo + 40 * c0];
        fft8_i(v);
        tw7_i(v + 1, tw64);
#pragma unroll
        for (int l0 = 0; l0 < 8; ++l0) E2[rho * E_RS + lo * 9 + l0] = v[l0];
      }
      if (__builtin_expect(__builtin_amdgcn_readfirstlane(sv[0] | sv[1] | sv[2] | sv[3] | s4) == 0, 1)) break;
#pragma unroll 1
      for (int u = 0; u < 161; ++u) {
        if (j < 4) unit(u);
        else { BAR(); BAR(); }
        BAR();
      }
    }
    BAR();
    {
      f2 v[8];
#pragma unroll
      for (int c = 0; c < 8; ++c) v[c] = E2[rho * E_RS + c * 9 + lo];
      fft8_i(v);
#pragma unroll
      for (int l1 = 0; l1 < 8; ++l1) E1[rho * E_RS + lo + 8 * l1] = v[l1];
    }
  } else {
    const int hs = (j - 5) * 160 + bid;
    W1Regs R;
    if (bid < 161) {
      BAR();
      act_clear(ACT, 2048 + s0 * 16 + j * 2, lane);
      act_clear(ACT, 2048 + s0 * 16 + j * 2 + 1, lane);
    }
    if (bid < 160) w2_load(R, hs, lane, wd);
    static_for<0, 5>([&](auto I) { gload1<I * 256>(raw[I], (s * 320 + lane) * 4, x); });
    if (bid < 161) BAR();
    for (;;) {
      BAR();
      const u4 sv = *reinterpret_cast<const u4*>(stat + 4);
      const unsigned s4 = stat[8];
      if (__builtin_amdgcn_readfirstlane(sv[0] | sv[1] | sv[2] | sv[3] | s4) == 0) break;
#pragma unroll 1
      for (int u = 0; u < 161; ++u) { BAR(); BAR(); BAR(); }
    }
    BAR();
    wait_vm<0>();
    if (bid < 160) w2_store(R, hs, lane, hst + (j - 5) * 512, W2F);
  }
  {
    const float lf = (float)lane;
#pragma unroll
    for (int k = 0; k < 4; ++k) { tw4[k] = cis(lf * ((k + 1) / 320.f)); tie(tw4[k]); }
  }
  BAR();
  STAMP(4);
  {
    f2 z[5];
#pragma unroll
    for (int k = 0; k < 5; ++k) z[k] = E1[(j * 5 + k) * E_RS + lane];
    tw4_i(z + 1, tw4);
    fft5_i(z);
#pragma unroll
    for (int i = 0; i < 5; ++i) tie(raw[i]);
    static_for<0, 5>([&](auto I) {
      f2 h = unpack_bf16(raw[I]) + bf16r2(z[I]);
      unsigned hp = pack_bf16(h.x, h.y);
      gstore1<I * 256>((s * 320 + lane) * 4, hp, Hout);
      z[I] = unpack_bf16(hp);
    });
    layernorm_row(z);
    const int mu = s0 >> 1, m = (s0 & 1) * 8 + j;
    const unsigned ao = (((mu * 20 + (lane >> 4)) * 64 + m + 16 * ((lane >> 2) & 3)) * 4 + (lane & 3)) * 4;
    static_for<0, 5>([&](auto I) { gstore1<(I & 1) * 4096 - 4096>(ao, pack_bf16(z[I].x, z[I].y), AF + 1024 + (I >> 1) * 2048); });
  }
  STAMP(5);
  STAMP_OUT(dbg, 1);
}

template <bool NEW> __device__ __forceinline__ void mfma_vv(f4& acc, const u4& a, const u4& b) {
  if constexpr (NEW) asm volatile("v_mfma_f32_16x16x32_bf16 %0, %1, %2, 0" : "=&v"(acc) : "v"(a), "v"(b));
  else asm volatile("v_mfma_f32_16x16x32_bf16 %0, %1, %2, %0" : "+v"(acc) : "v"(a), "v"(b));
}
template <bool NEW> __device__ __forceinline__ void mfma_av(f4& acc, const u4& a, const u4& b) {
  if constexpr (NEW) asm volatile("v_mfma_f32_16x16x32_bf16 %0, %1, %2, 0" : "=&v"(acc) : "a"(a), "v"(b));
  else asm volatile("v_mfma_f32_16x16x32_bf16 %0, %1, %2, %0" : "+v"(acc) : "a"(a), "v"(b));
}
template <int MOFF, int OFF> __device__ __forceinline__ void glds(unsigned m0base, unsigned voff, const void* base) {
  asm volatile("s_add_u32 m0, %0, %c3\n\ts_nop 0\n\tglobal_load_lds_dwordx4 %1, %2 offset:%c4"
               ::"s"(m0base), "v"(voff), "s"(base), "n"(MOFF), "n"(OFF) : "memory", "scc");
}
template <int OFF> __device__ __forceinline__ void lds_read(u4& d, unsigned addr) {
  asm volatile("ds_read_b128 %0, %1 offset:%c2" : "=v"(d) : "v"(addr), "n"(OFF) : "memory");
}
__device__ __forceinline__ f2 geglu2(f2 g, f2 u) {
  const float c1 = -1.4426950408889634f * 1.5957691216057308f, c2 = -1.4426950408889634f * 0.07135481627260024f;
  f2 w = g * (g * g * c2 + c1);
  f2 d = {1.f + __builtin_amdgcn_exp2f(w.x), 1.f + __builtin_amdgcn_exp2f(w.y)};
  f2 r = {__builtin_amdgcn_rcpf(d.x), __builtin_amdgcn_rcpf(d.y)};
  return g * u * r;
}

template <int OFF> __device__ __forceinline__ void gload_vc(u4& d, unsigned voff, const void* base) {
  asm volatile("global_load_dwordx4 %0, %1, %2 offset:%c3 sc1" : "=v"(d) : "v"(voff), "s"(base), "n"(OFF) : "memory");
}
__device__ __forceinline__ void gload1c(unsigned& d, unsigned voff, const void* base) {
  asm volatile("global_load_dword %0, %1, %2 sc1" : "=v"(d) : "v"(voff), "s"(base) : "memory");
}
template <int OFF> __device__ __forceinline__ void lds_write(unsigned addr, unsigned v) {
  asm volatile("ds_write_b32 %0, %1 offset:%c2" ::"v"(addr), "v"(v), "n"(OFF) : "memory");
}
template <int OFF> __device__ __forceinline__ void lds_write4(unsigned addr, f4 v) {
  asm volatile("ds_write_b128 %0, %1 offset:%c2" ::"v"(addr), "v"(v), "n"(OFF) : "memory");
}
__device__ __forceinline__ void v_mul(float& d, float a, float b) { d = a * b; tie(d); }
__device__ __forceinline__ void v_mul2(float& d, float a) { d = d * a; tie(d); }
__device__ __forceinline__ void v_fmas(float& d, float s, float c) { d = __builtin_fmaf(d, s, c); tie(d); }
__device__ __forceinline__ void v_exp(float& d) { d = __builtin_amdgcn_exp2f(d); tie(d); }
__device__ __forceinline__ void v_rcp(float& d) { d = __builtin_amdgcn_rcpf(d); tie(d); }
__device__ __forceinline__ void v_add1(float& d) { d = 1.f + d; tie(d); }
__device__ __forceinline__ void v_cvt(unsigned& d, float a, float b) { d = min(pack_bf16(a, b), 0xfffffffeu); tie(d); }
__device__ __forceinline__ void v_add3(float& d, float a, float b) { d = a + b; tie(d); }
__device__ __forceinline__ void v_add2(float& d, float a) { d = d + a; tie(d); }
__device__ __forceinline__ void v_pack(unsigned& d, float a, float b) { d = pack_bf16(a, b); tie(d); }
__device__ __forceinline__ void v_lo(float& d, unsigned a) { d = bflo(a); tie(d); }
__device__ __forceinline__ void v_hi(float& d, unsigned a) { d = bfhi(a); tie(d); }
template <int OFF> __device__ __forceinline__ void lds_readf(f4& d, unsigned addr) {
  asm volatile("ds_read_b128 %0, %1 offset:%c2" : "=v"(d) : "v"(addr), "n"(OFF) : "memory");
}
__device__ __forceinline__ u2 fin(f4 a, f4 b, f4 c, f4 d, u2 h) {
  f4 sum = (a + b) + (c + d);
  f2 o0 = unpack_bf16(h.x) + bf16r2(f2{sum[0], sum[1]});
  f2 o1 = unpack_bf16(h.y) + bf16r2(f2{sum[2], sum[3]});
  return u2{pack_bf16(o0.x, o0.y), pack_bf16(o1.x, o1.y)};
}
#define SB() __builtin_amdgcn_sched_barrier(0)
__device__ __forceinline__ void v_max3(unsigned& d, unsigned a, unsigned b) { asm volatile("v_max3_u32 %0, %0, %1, %2" : "+v"(d) : "v"(a), "v"(b)); }
#define SPIN_CAP 4096
__device__ __forceinline__ void gstore4h(unsigned voff, u4 d, void* base) {
  asm volatile("s_mov_b64 exec, 0xffffffff\n\tglobal_store_dwordx4 %0, %1, %2 sc1\n\ts_mov_b64 exec, -1\n\ts_nop 0" ::"v"(voff), "v"(d), "s"(base) : "memory");
}
__device__ __forceinline__ void gate(unsigned& fl, unsigned fo, const void* base, unsigned long long mask) {
  unsigned cnt;
  asm volatile("s_movk_i32 %1, %c5\n1:\n\tv_cmp_eq_u32 vcc, -1, %0\n\ts_and_b64 vcc, vcc, %4\n\ts_cbranch_scc0 2f\n\ts_sub_u32 %1, %1, 1\n\ts_cbranch_scc1 2f\n\t"
               "global_load_dword %0, %2, %3 sc1\n\ts_waitcnt vmcnt(0)\n\ts_branch 1b\n2:"
               : "+v"(fl), "=&s"(cnt) : "v"(fo), "s"(base), "s"(mask), "n"(SPIN_CAP) : "vcc", "scc", "memory");
}
__device__ __forceinline__ void act_redo(const u4* __restrict__ AF, const u4* __restrict__ W1F, u4* __restrict__ ACT, int mt0, int cb0, int lane) {
  typedef __bf16 bf8 __attribute__((ext_vector_type(8)));
#pragma unroll 1
  for (int it = 0; it < 48; ++it) {
    const int q = it / 12, kk = it - q * 12, mt = mt0 + 2 * q, cb = cb0 + kk / 3, t = kk % 3;
    unsigned r[4];
#pragma unroll
    for (int w = 0; w < 4; ++w) {
      f4 acc = {0.f, 0.f, 0.f, 0.f};
#pragma unroll 1
      for (int k = 0; k < 20; ++k) {
        u4 a = AF[(mt * 20 + k) * 64 + lane], b = W1F[(((cb * 4 + w) * 20 + k) * 3 + t) * 64 + lane];
        acc = __builtin_amdgcn_mfma_f32_16x16x32_bf16(__builtin_bit_cast(bf8, b), __builtin_bit_cast(bf8, a), acc, 0, 0, 0);
      }
      f2 o = geglu2(f2{acc[0], acc[1]}, f2{acc[2], acc[3]});
      r[w] = min(pack_bf16(o.x, o.y), 0xfffffffeu);
    }
    const int g = lane >> 4;
    const unsigned p0 = (g & 1) ? r[1] : r[0], p1 = (g & 1) ? r[0] : r[1], p2 = (g & 1) ? r[3] : r[2], p3 = (g & 1) ? r[2] : r[3];
    const u4 o = (g & 2) ? u4{p2, p3, p0, p1} : u4{p0, p1, p2, p3};
    gstore4c<0>(((mt * 48 + cb * 3 + t) * 64 + lane) * 16, o, ACT);
  }
  wait_vm<0>();
}

extern "C" __global__ __launch_bounds__(256, 1) void k4_kernel(const u4* __restrict__ AF, const u4* __restrict__ W1F, u4* __restrict__ ACT,
                                                    const u4* __restrict__ W2F, const u2* __restrict__ Hin, u2* __restrict__ out DBG_ARG) {
  __shared__ __attribute__((aligned(16))) u4 lds[10240];
  STAMP_DECL
  STAMP(0);
  int bid = blockIdx.x, lane = threadIdx.x & 63;
  int w = __builtin_amdgcn_readfirstlane(threadIdx.x >> 6);
  const char* abase = reinterpret_cast<const char*>(AF) + (size_t)((bid & 1) * 8 + ((bid >> 3) & 7)) * 163840 + w * 40960;
  const char* bbase = reinterpret_cast<const char*>(W1F) + (size_t)((((bid & 7) >> 1) * 4 + (bid >> 6)) * 4 + w) * 61440;
  const char* wbase = reinterpret_cast<const char*>(W2F) + ((size_t)((bid >> 5) * 5) * 48 + w * 12) * 1024 + 4096;
  const unsigned lbase = (unsigned)(size_t)(__attribute__((address_space(3))) void*)lds;
  const unsigned m0b = lbase + w * 40960, vo = lane * 16;
  const unsigned la0 = lbase + vo, la1 = la0 + 61440, la2 = la0 + 122880;
  u4 wd[12][5];
  u2 h[6];
  unsigned fl[2], mx[2] = {0, 0};
  f4 ac2[4][5];
    u4 a[8], at[2][8], B[20][3], st[2], E[4][12];
    f4 acc[8][3];
    float gt[3][2], gu[3][2];
    unsigned gp[3];
    f4 R[4][4];
    float X[4][4], Y[4][4], Z[4][4];
    unsigned op[4][2];
    asm volatile("s_nop 4" ::: "memory");
    glds<0, 0>(m0b, vo, abase + 0);
    glds<20480, 0>(m0b, vo, abase + 20480);
    glds<0, 1024>(m0b, vo, abase + 0);
    glds<20480, 1024>(m0b, vo, abase + 20480);
    glds<0, 2048>(m0b, vo, abase + 0);
    glds<20480, 2048>(m0b, vo, abase + 20480);
    gload_a<-4096>(B[0][0], vo, bbase + 4096);
    gload_a<-3072>(B[0][1], vo, bbase + 4096);
    gload_a<-2048>(B[0][2], vo, bbase + 4096);
    gload_a<-1024>(B[1][0], vo, bbase + 4096);
    gload_a<0>(B[1][1], vo, bbase + 4096);
    gload_a<1024>(B[1][2], vo, bbase + 4096);
    glds<0, 3072>(m0b, vo, abase + 0);
    glds<20480, 3072>(m0b, vo, abase + 20480);
    glds<4096, 0>(m0b, vo, abase + 4096);
    glds<24576, 0>(m0b, vo, abase + 24576);
    gload_v<2048>(B[2][0], vo, bbase + 4096);
    gload_v<3072>(B[2][1], vo, bbase + 4096);
    gload_v<-4096>(B[2][2], vo, bbase + 12288);
    gload_v<-3072>(B[3][0], vo, bbase + 12288);
    gload_v<-2048>(B[3][1], vo, bbase + 12288);
    gload_v<-1024>(B[3][2], vo, bbase + 12288);
    glds<4096, 1024>(m0b, vo, abase + 4096);
    glds<24576, 1024>(m0b, vo, abase + 24576);
    glds<4096, 2048>(m0b, vo, abase + 4096);
    glds<24576, 2048>(m0b, vo, abase + 24576);
    gload_v<0>(B[4][0], vo, bbase + 12288);
    gload_v<1024>(B[4][1], vo, bbase + 12288);
    gload_v<2048>(B[4][2], vo, bbase + 12288);
    gload_v<3072>(B[5][0], vo, bbase + 12288);
    gload_v<-4096>(B[5][1], vo, bbase + 20480);
    gload_v<-3072>(B[5][2], vo, bbase + 20480);
    glds<4096, 3072>(m0b, vo, abase + 4096);
    glds<24576, 3072>(m0b, vo, abase + 24576);
    glds<8192, 0>(m0b, vo, abase + 8192);
    glds<28672, 0>(m0b, vo, abase + 28672);
    gload_v<-2048>(B[6][0], vo, bbase + 20480);
    gload_v<-1024>(B[6][1], vo, bbase + 20480);
    gload_v<0>(B[6][2], vo, bbase + 20480);
    wait_vm<37>();
    asm volatile("s_barrier" ::: "memory");
    lds_read<0>(a[0], la0);
    lds_read<20480>(a[1], la0);
    lds_read<40960>(a[2], la0);
    lds_read<0>(a[3], la1);
    lds_read<20480>(a[4], la1);
    lds_read<40960>(a[5], la1);
    lds_read<0>(a[6], la2);
    lds_read<20480>(a[7], la2);
    wait_vm<30>();
    asm volatile("s_barrier" ::: "memory");
    wait_lgkm<7>();
    mfma_av<true>(acc[0][0], B[0][0], a[0]);
    mfma_av<true>(acc[0][1], B[0][1], a[0]);
    mfma_av<true>(acc[0][2], B[0][2], a[0]);
    lds_read<1024>(a[0], la0);
    gload_v<1024>(B[7][0], vo, bbase + 20480);
    wait_lgkm<7>();
    mfma_av<true>(acc[1][0], B[0][0], a[1]);
    mfma_av<true>(acc[1][1], B[0][1], a[1]);
    mfma_av<true>(acc[1][2], B[0][2], a[1]);
    lds_read<21504>(a[1], la0);
    gload_v<2048>(B[7][1], vo, bbase + 20480);
    wait_lgkm<7>();
    mfma_av<true>(acc[2][0], B[0][0], a[2]);
    mfma_av<true>(acc[2][1], B[0][1], a[2]);
    mfma_av<true>(acc[2][2], B[0][2], a[2]);
    lds_read<41984>(a[2], la0);
    gload_v<3072>(B[7][2], vo, bbase + 20480);
    wait_lgkm<7>();
    mfma_av<true>(acc[3][0], B[0][0], a[3]);
    mfma_av<true>(acc[3][1], B[0][1], a[3]);
    mfma_av<true>(acc[3][2], B[0][2], a[3]);
    lds_read<1024>(a[3], la1);
    glds<8192, 1024>(m0b, vo, abase + 8192);
    wait_lgkm<7>();
    mfma_av<true>(acc[4][0], B[0][0], a[4]);
    mfma_av<true>(acc[4][1], B[0][1], a[4]);
    mfma_av<true>(acc[4][2], B[0][2], a[4]);
    lds_read<21504>(a[4], la1);
    glds<28672, 1024>(m0b, vo, abase + 28672);
    wait_lgkm<7>();
    mfma_av<true>(acc[5][0], B[0][0], a[5]);
    mfma_av<true>(acc[5][1], B[0][1], a[5]);
    mfma_av<true>(acc[5][2], B[0][2], a[5]);
    lds_read<41984>(a[5], la1);
    glds<8192, 2048>(m0b, vo, abase + 8192);
    wait_lgkm<7>();
    mfma_av<true>(acc[6][0], B[0][0], a[6]);
    mfma_av<true>(acc[6][1], B[0][1], a[6]);
    mfma_av<true>(acc[6][2], B[0][2], a[6]);
    lds_read<1024>(a[6], la2);
    glds<28672, 2048>(m0b, vo, abase + 28672);
    wait_lgkm<7>();
    mfma_av<true>(acc[7][0], B[0][0], a[7]);
    mfma_av<true>(acc[7][1], B[0][1], a[7]);
    mfma_av<true>(acc[7][2], B[0][2], a[7]);
    lds_read<21504>(a[7], la2);
    wait_vm<34>();
    wait_lgkm<7>();
    mfma_av<false>(acc[0][0], B[1][0], a[0]);
    mfma_av<false>(acc[0][1], B[1][1], a[0]);
    mfma_av<false>(acc[0][2], B[1][2], a[0]);
    lds_read<2048>(a[0], la0);
    gload_a<-4096>(B[8][0], vo, bbase + 28672);
    wait_lgkm<7>();
    mfma_av<false>(acc[1][0], B[1][0], a[1]);
    mfma_av<false>(acc[1][1], B[1][1], a[1]);
    mfma_av<false>(acc[1][2], B[1][2], a[1]);
    lds_read<22528>(a[1], la0);
    gload_a<-3072>(B[8][1], vo, bbase + 28672);
    wait_lgkm<7>();
    mfma_av<false>(acc[2][0], B[1][0], a[2]);
    mfma_av<false>(acc[2][1], B[1][1], a[2]);
    mfma_av<false>(acc[2][2], B[1][2], a[2]);
    lds_read<43008>(a[2], la0);
    gload_a<-2048>(B[8][2], vo, bbase + 28672);
    wait_lgkm<7>();
    mfma_av<false>(acc[3][0], B[1][0], a[3]);
    mfma_av<false>(acc[3][1], B[1][1], a[3]);
    mfma_av<false>(acc[3][2], B[1][2], a[3]);
    lds_read<2048>(a[3], la1);
    gload_a<-1024>(B[9][0], vo, bbase + 28672);
  asm volatile("" : "+s"(bid), "+s"(w));
  tie(lane);
  const int xcd = bid & 7, loc = bid >> 3;
  const int rb = (xcd & 1) * 8 + (loc & 7), cb = (xcd >> 1) * 4 + (loc >> 3);
  const int rb5 = xcd * 4 + (loc & 3), cb5 = loc >> 2;
  const int mt0 = (rb5 >> 1) * 8 + (rb5 & 1);
  char* act = reinterpret_cast<char*>(ACT) + ((size_t)(rb * 8) * 48 + cb * 3) * 1024;
  const char* gbase = reinterpret_cast<const char*>(ACT) + ((size_t)mt0 * 48 + w * 12) * 1024;
#define QROT 1
  const int sw = QROT == 2 ? cb5 & 1 : QROT == 3 ? cb5 % 3 : 0;
  int tq[4];
  const char* eb[4];
#pragma unroll
  for (int p = 0; p < 4; ++p) {
    tq[p] = p < QROT ? (p + sw >= QROT ? p + sw - QROT : p + sw) : p;
    eb[p] = gbase + 4096 + tq[p] * 98304;
  }
    wait_lgkm<7>();
    mfma_av<false>(acc[4][0], B[1][0], a[4]);
    mfma_av<false>(acc[4][1], B[1][1], a[4]);
    mfma_av<false>(acc[4][2], B[1][2], a[4]);
    lds_read<22528>(a[4], la1);
    gload_a<0>(B[9][1], vo, bbase + 28672);
    wait_lgkm<7>();
    mfma_av<false>(acc[5][0], B[1][0], a[5]);
    mfma_av<false>(acc[5][1], B[1][1], a[5]);
    mfma_av<false>(acc[5][2], B[1][2], a[5]);
    lds_read<43008>(a[5], la1);
    gload_a<1024>(B[9][2], vo, bbase + 28672);
    wait_lgkm<7>();
    mfma_av<false>(acc[6][0], B[1][0], a[6]);
    mfma_av<false>(acc[6][1], B[1][1], a[6]);
    mfma_av<false>(acc[6][2], B[1][2], a[6]);
    lds_read<2048>(a[6], la2);
    glds<8192, 3072>(m0b, vo, abase + 8192);
    wait_lgkm<7>();
    mfma_av<false>(acc[7][0], B[1][0], a[7]);
    mfma_av<false>(acc[7][1], B[1][1], a[7]);
    mfma_av<false>(acc[7][2], B[1][2], a[7]);
    lds_read<22528>(a[7], la2);
    wait_vm<34>();
    asm volatile("s_barrier" ::: "memory");
    wait_lgkm<7>();
    mfma_vv<false>(acc[0][0], B[2][0], a[0]);
    mfma_vv<false>(acc[0][1], B[2][1], a[0]);
    mfma_vv<false>(acc[0][2], B[2][2], a[0]);
    lds_read<3072>(a[0], la0);
    glds<28672, 3072>(m0b, vo, abase + 28672);
    wait_lgkm<7>();
    mfma_vv<false>(acc[1][0], B[2][0], a[1]);
    mfma_vv<false>(acc[1][1], B[2][1], a[1]);
    mfma_vv<false>(acc[1][2], B[2][2], a[1]);
    lds_read<23552>(a[1], la0);
    glds<12288, 0>(m0b, vo, abase + 12288);
    wait_lgkm<7>();
    mfma_vv<false>(acc[2][0], B[2][0], a[2]);
    mfma_vv<false>(acc[2][1], B[2][1], a[2]);
    mfma_vv<false>(acc[2][2], B[2][2], a[2]);
    lds_read<44032>(a[2], la0);
    glds<32768, 0>(m0b, vo, abase + 32768);
    wait_lgkm<7>();
    mfma_vv<false>(acc[3][0], B[2][0], a[3]);
    mfma_vv<false>(acc[3][1], B[2][1], a[3]);
    mfma_vv<false>(acc[3][2], B[2][2], a[3]);
    lds_read<3072>(a[3], la1);
    gload_a<2048>(B[10][0], vo, bbase + 28672);
    wait_lgkm<7>();
    mfma_vv<false>(acc[4][0], B[2][0], a[4]);
    mfma_vv<false>(acc[4][1], B[2][1], a[4]);
    mfma_vv<false>(acc[4][2], B[2][2], a[4]);
    lds_read<23552>(a[4], la1);
    gload_a<3072>(B[10][1], vo, bbase + 28672);
    wait_lgkm<7>();
    mfma_vv<false>(acc[5][0], B[2][0], a[5]);
    mfma_vv<false>(acc[5][1], B[2][1], a[5]);
    mfma_vv<false>(acc[5][2], B[2][2], a[5]);
    lds_read<44032>(a[5], la1);
    gload_a<-4096>(B[10][2], vo, bbase + 36864);
    wait_lgkm<7>();
    mfma_vv<false>(acc[6][0], B[2][0], a[6]);
    mfma_vv<false>(acc[6][1], B[2][1], a[6]);
    mfma_vv<false>(acc[6][2], B[2][2], a[6]);
    lds_read<3072>(a[6], la2);
    gload_a<-3072>(B[11][0], vo, bbase + 36864);
    wait_lgkm<7>();
    mfma_vv<false>(acc[7][0], B[2][0], a[7]);
    mfma_vv<false>(acc[7][1], B[2][1], a[7]);
    mfma_vv<false>(acc[7][2], B[2][2], a[7]);
    lds_read<23552>(a[7], la2);
    wait_vm<38>();
    wait_lgkm<7>();
    mfma_vv<false>(acc[0][0], B[3][0], a[0]);
    mfma_vv<false>(acc[0][1], B[3][1], a[0]);
    mfma_vv<false>(acc[0][2], B[3][2], a[0]);
    lds_read<4096>(a[0], la0);
    gload_a<-2048>(B[11][1], vo, bbase + 36864);
    wait_lgkm<7>();
    mfma_vv<false>(acc[1][0], B[3][0], a[1]);
    mfma_vv<false>(acc[1][1], B[3][1], a[1]);
    mfma_vv<false>(acc[1][2], B[3][2], a[1]);
    lds_read<24576>(a[1], la0);
    gload_a<-1024>(B[11][2], vo, bbase + 36864);
    wait_lgkm<7>();
    mfma_vv<false>(acc[2][0], B[3][0], a[2]);
    mfma_vv<false>(acc[2][1], B[3][1], a[2]);
    mfma_vv<false>(acc[2][2], B[3][2], a[2]);
    lds_read<45056>(a[2], la0);
    glds<12288, 1024>(m0b, vo, abase + 12288);
    wait_lgkm<7>();
    mfma_vv<false>(acc[3][0], B[3][0], a[3]);
    mfma_vv<false>(acc[3][1], B[3][1], a[3]);
    mfma_vv<false>(acc[3][2], B[3][2], a[3]);
    lds_read<4096>(a[3], la1);
    glds<32768, 1024>(m0b, vo, abase + 32768);
  asm volatile("" : "+s"(w));
  tie(lane);
  const unsigned lw0 = la0 + (w ^ (lane >> 4)) * 4, lw1 = lw0 + 61440, lw2 = lw0 + 122880;
  const unsigned hoff = (1 + (w >> 1)) * 1024 + (w & 1) * 512 + (lane & 31) * 16;
  const unsigned ls0a = la0 + (w == 3 ? 20480 : w * 1024), ls0b = ls0a + 81920, ls1a = lbase + 20480 + hoff, ls1b = ls1a + 81920, lr = la0 + w * 1024;
  const unsigned so0 = vo + (w == 3 ? 49152 : w * 1024), so1 = 49152 + hoff;
    wait_lgkm<7>();
    mfma_vv<false>(acc[4][0], B[3][0], a[4]);
    mfma_vv<false>(acc[4][1], B[3][1], a[4]);
    mfma_vv<false>(acc[4][2], B[3][2], a[4]);
    lds_read<24576>(a[4], la1);
    glds<12288, 2048>(m0b, vo, abase + 12288);
    wait_lgkm<7>();
    mfma_vv<false>(acc[5][0], B[3][0], a[5]);
    mfma_vv<false>(acc[5][1], B[3][1], a[5]);
    mfma_vv<false>(acc[5][2], B[3][2], a[5]);
    lds_read<45056>(a[5], la1);
    glds<32768, 2048>(m0b, vo, abase + 32768);
    wait_lgkm<7>();
    mfma_vv<false>(acc[6][0], B[3][0], a[6]);
    mfma_vv<false>(acc[6][1], B[3][1], a[6]);
    mfma_vv<false>(acc[6][2], B[3][2], a[6]);
    lds_read<4096>(a[6], la2);
    glds<12288, 3072>(m0b, vo, abase + 12288);
    wait_lgkm<7>();
    mfma_vv<false>(acc[7][0], B[3][0], a[7]);
    mfma_vv<false>(acc[7][1], B[3][1], a[7]);
    mfma_vv<false>(acc[7][2], B[3][2], a[7]);
    lds_read<24576>(a[7], la2);
    wait_vm<38>();
    asm volatile("s_barrier" ::: "memory");
    wait_lgkm<7>();
    mfma_vv<false>(acc[0][0], B[4][0], a[0]);
    mfma_vv<false>(acc[0][1], B[4][1], a[0]);
    mfma_vv<false>(acc[0][2], B[4][2], a[0]);
    lds_read<5120>(a[0], la0);
    glds<32768, 3072>(m0b, vo, abase + 32768);
    wait_lgkm<7>();
    mfma_vv<false>(acc[1][0], B[4][0], a[1]);
    mfma_vv<false>(acc[1][1], B[4][1], a[1]);
    mfma_vv<false>(acc[1][2], B[4][2], a[1]);
    lds_read<25600>(a[1], la0);
    glds<16384, 0>(m0b, vo, abase + 16384);
    wait_lgkm<7>();
    mfma_vv<false>(acc[2][0], B[4][0], a[2]);
    mfma_vv<false>(acc[2][1], B[4][1], a[2]);
    mfma_vv<false>(acc[2][2], B[4][2], a[2]);
    lds_read<46080>(a[2], la0);
    glds<36864, 0>(m0b, vo, abase + 36864);
    wait_lgkm<7>();
    mfma_vv<false>(acc[3][0], B[4][0], a[3]);
    mfma_vv<false>(acc[3][1], B[4][1], a[3]);
    mfma_vv<false>(acc[3][2], B[4][2], a[3]);
    lds_read<5120>(a[3], la1);
    glds<16384, 1024>(m0b, vo, abase + 16384);
    wait_lgkm<7>();
    mfma_vv<false>(acc[4][0], B[4][0], a[4]);
    mfma_vv<false>(acc[4][1], B[4][1], a[4]);
    mfma_vv<false>(acc[4][2], B[4][2], a[4]);
    lds_read<25600>(a[4], la1);
    glds<36864, 1024>(m0b, vo, abase + 36864);
    wait_lgkm<7>();
    mfma_vv<false>(acc[5][0], B[4][0], a[5]);
    mfma_vv<false>(acc[5][1], B[4][1], a[5]);
    mfma_vv<false>(acc[5][2], B[4][2], a[5]);
    lds_read<46080>(a[5], la1);
    glds<16384, 2048>(m0b, vo, abase + 16384);
    wait_lgkm<7>();
    mfma_vv<false>(acc[6][0], B[4][0], a[6]);
    mfma_vv<false>(acc[6][1], B[4][1], a[6]);
    mfma_vv<false>(acc[6][2], B[4][2], a[6]);
    lds_read<5120>(a[6], la2);
    glds<36864, 2048>(m0b, vo, abase + 36864);
    wait_lgkm<7>();
    mfma_vv<false>(acc[7][0], B[4][0], a[7]);
    mfma_vv<false>(acc[7][1], B[4][1], a[7]);
    mfma_vv<false>(acc[7][2], B[4][2], a[7]);
    lds_read<25600>(a[7], la2);
    wait_vm<42>();
    wait_lgkm<7>();
    mfma_vv<false>(acc[0][0], B[5][0], a[0]);
    mfma_vv<false>(acc[0][1], B[5][1], a[0]);
    mfma_vv<false>(acc[0][2], B[5][2], a[0]);
    lds_read<6144>(a[0], la0);
    glds<16384, 3072>(m0b, vo, abase + 16384);
    wait_lgkm<7>();
    mfma_vv<false>(acc[1][0], B[5][0], a[1]);
    mfma_vv<false>(acc[1][1], B[5][1], a[1]);
    mfma_vv<false>(acc[1][2], B[5][2], a[1]);
    lds_read<26624>(a[1], la0);
    glds<36864, 3072>(m0b, vo, abase + 36864);
    wait_lgkm<7>();
    mfma_vv<false>(acc[2][0], B[5][0], a[2]);
    mfma_vv<false>(acc[2][1], B[5][1], a[2]);
    mfma_vv<false>(acc[2][2], B[5][2], a[2]);
    lds_read<47104>(a[2], la0);
    gload_a<0>(B[12][0], vo, bbase + 36864);
    wait_lgkm<7>();
    mfma_vv<false>(acc[3][0], B[5][0], a[3]);
    mfma_vv<false>(acc[3][1], B[5][1], a[3]);
    mfma_vv<false>(acc[3][2], B[5][2], a[3]);
    lds_read<6144>(a[3], la1);
    gload_a<1024>(B[12][1], vo, bbase + 36864);
  asm volatile("" : "+s"(w));
  tie(lane);
  const unsigned ob = ((2 * mt0 + ((lane & 15) >> 3) + 256 * (lane & 7)) * 160 + cb5 * 20) * 8;
  unsigned ou[6], lu[6];
#pragma unroll
  for (int j = 0; j < 5; ++j) {
    const int u = j < 4 ? min(w * 4 + j, 14) : 15 + w, q = (u * 13) >> 6;
    ou[j] = ob + (lane >> 4) * 8 + (q == 0 ? tq[0] : q == 1 ? tq[1] : q == 2 ? tq[2] : 3) * 5120 + (u - q * 5) * 32;
  }
  ou[5] = ob + w * 8 + 3 * 5120 + 4 * 32;
  auto lu_init = [&] {
    int l2 = lane, w2 = w;
    asm volatile("" : "+s"(w2));
    tie(l2);
#pragma unroll
    for (int j = 0; j < 5; ++j) lu[j] = lbase + l2 * 16 + (j < 4 ? min(w2 * 4 + j, 14) : 15 + w2) * 4096;
    lu[5] = lbase + 19 * 4096 + (w2 * 16 + (l2 & 15)) * 16;
  };
  const int gl = lane < 48 ? lane : lane - 48, gh = gl >= 24;
  const unsigned fo = gh * 98304 + (gl - gh * 24) * 512 + 508;
  const float gc1 = -1.4426950408889634f * 1.5957691216057308f, gc2 = -1.4426950408889634f * 0.07135481627260024f;
    wait_lgkm<7>();
    mfma_vv<false>(acc[4][0], B[5][0], a[4]);
    mfma_vv<false>(acc[4][1], B[5][1], a[4]);
    mfma_vv<false>(acc[4][2], B[5][2], a[4]);
    lds_read<26624>(a[4], la1);
    gload_a<2048>(B[12][2], vo, bbase + 36864);
    wait_lgkm<7>();
    mfma_vv<false>(acc[5][0], B[5][0], a[5]);
    mfma_vv<false>(acc[5][1], B[5][1], a[5]);
    mfma_vv<false>(acc[5][2], B[5][2], a[5]);
    lds_read<47104>(a[5], la1);
    gload_a<3072>(B[13][0], vo, bbase + 36864);
    wait_lgkm<7>();
    mfma_vv<false>(acc[6][0], B[5][0], a[6]);
    mfma_vv<false>(acc[6][1], B[5][1], a[6]);
    mfma_vv<false>(acc[6][2], B[5][2], a[6]);
    lds_read<6144>(a[6], la2);
    gload_a<-4096>(B[13][1], vo, bbase + 45056);
    wait_lgkm<7>();
    mfma_vv<false>(acc[7][0], B[5][0], a[7]);
    mfma_vv<false>(acc[7][1], B[5][1], a[7]);
    mfma_vv<false>(acc[7][2], B[5][2], a[7]);
    lds_read<26624>(a[7], la2);
    wait_vm<42>();
    asm volatile("s_barrier" ::: "memory");
    wait_lgkm<7>();
    mfma_vv<false>(acc[0][0], B[6][0], a[0]);
    mfma_vv<false>(acc[0][1], B[6][1], a[0]);
    mfma_vv<false>(acc[0][2], B[6][2], a[0]);
    lds_read<7168>(a[0], la0);
    gload_a<-3072>(B[13][2], vo, bbase + 45056);
    wait_lgkm<7>();
    mfma_vv<false>(acc[1][0], B[6][0], a[1]);
    mfma_vv<false>(acc[1][1], B[6][1], a[1]);
    mfma_vv<false>(acc[1][2], B[6][2], a[1]);
    lds_read<27648>(a[1], la0);
    gload_v<-2048>(B[14][0], vo, bbase + 45056);
    wait_lgkm<7>();
    mfma_vv<false>(acc[2][0], B[6][0], a[2]);
    mfma_vv<false>(acc[2][1], B[6][1], a[2]);
    mfma_vv<false>(acc[2][2], B[6][2], a[2]);
    lds_read<48128>(a[2], la0);
    gload_v<-1024>(B[14][1], vo, bbase + 45056);
    wait_lgkm<7>();
    mfma_vv<false>(acc[3][0], B[6][0], a[3]);
    mfma_vv<false>(acc[3][1], B[6][1], a[3]);
    mfma_vv<false>(acc[3][2], B[6][2], a[3]);
    lds_read<7168>(a[3], la1);
    gload_v<0>(B[14][2], vo, bbase + 45056);
    wait_lgkm<7>();
    mfma_vv<false>(acc[4][0], B[6][0], a[4]);
    mfma_vv<false>(acc[4][1], B[6][1], a[4]);
    mfma_vv<false>(acc[4][2], B[6][2], a[4]);
    lds_read<27648>(a[4], la1);
    gload_v<1024>(B[15][0], vo, bbase + 45056);
    wait_lgkm<7>();
    mfma_vv<false>(acc[5][0], B[6][0], a[5]);
    mfma_vv<false>(acc[5][1], B[6][1], a[5]);
    mfma_vv<false>(acc[5][2], B[6][2], a[5]);
    lds_read<48128>(a[5], la1);
    gload_v<2048>(B[15][1], vo, bbase + 45056);
    wait_lgkm<7>();
    mfma_vv<false>(acc[6][0], B[6][0], a[6]);
    mfma_vv<false>(acc[6][1], B[6][1], a[6]);
    mfma_vv<false>(acc[6][2], B[6][2], a[6]);
    lds_read<7168>(a[6], la2);
    gload_v<3072>(B[15][2], vo, bbase + 45056);
    wait_lgkm<7>();
    mfma_vv<false>(acc[7][0], B[6][0], a[7]);
    mfma_vv<false>(acc[7][1], B[6][1], a[7]);
    mfma_vv<false>(acc[7][2], B[6][2], a[7]);
    lds_read<27648>(a[7], la2);
    wait_vm<46>();
    wait_lgkm<7>();
    mfma_vv<false>(acc[0][0], B[7][0], a[0]);
    mfma_vv<false>(acc[0][1], B[7][1], a[0]);
    mfma_vv<false>(acc[0][2], B[7][2], a[0]);
    lds_read<8192>(a[0], la0);
    gload_v<-4096>(B[16][0], vo, bbase + 53248);
    wait_lgkm<7>();
    mfma_vv<false>(acc[1][0], B[7][0], a[1]);
    mfma_vv<false>(acc[1][1], B[7][1], a[1]);
    mfma_vv<false>(acc[1][2], B[7][2], a[1]);
    lds_read<28672>(a[1], la0);
    gload_v<-3072>(B[16][1], vo, bbase + 53248);
    wait_lgkm<7>();
    mfma_vv<false>(acc[2][0], B[7][0], a[2]);
    mfma_vv<false>(acc[2][1], B[7][1], a[2]);
    mfma_vv<false>(acc[2][2], B[7][2], a[2]);
    lds_read<49152>(a[2], la0);
    gload_v<-2048>(B[16][2], vo, bbase + 53248);
    wait_lgkm<7>();
    mfma_vv<false>(acc[3][0], B[7][0], a[3]);
    mfma_vv<false>(acc[3][1], B[7][1], a[3]);
    mfma_vv<false>(acc[3][2], B[7][2], a[3]);
    lds_read<8192>(a[3], la1);
    gload_v<-1024>(B[17][0], vo, bbase + 53248);
    wait_lgkm<7>();
    mfma_vv<false>(acc[4][0], B[7][0], a[4]);
    mfma_vv<false>(acc[4][1], B[7][1], a[4]);
    mfma_vv<false>(acc[4][2], B[7][2], a[4]);
    lds_read<28672>(a[4], la1);
    gload_v<0>(B[17][1], vo, bbase + 53248);
    wait_lgkm<7>();
    mfma_vv<false>(acc[5][0], B[7][0], a[5]);
    mfma_vv<false>(acc[5][1], B[7][1], a[5]);
    mfma_vv<false>(acc[5][2], B[7][2], a[5]);
    lds_read<49152>(a[5], la1);
    gload_v<1024>(B[17][2], vo, bbase + 53248);
    wait_lgkm<7>();
    mfma_vv<false>(acc[6][0], B[7][0], a[6]);
    mfma_vv<false>(acc[6][1], B[7][1], a[6]);
    mfma_vv<false>(acc[6][2], B[7][2], a[6]);
    lds_read<8192>(a[6], la2);
    gload_v<2048>(B[18][0], vo, bbase + 53248);
    wait_lgkm<7>();
    mfma_vv<false>(acc[7][0], B[7][0], a[7]);
    mfma_vv<false>(acc[7][1], B[7][1], a[7]);
    mfma_vv<false>(acc[7][2], B[7][2], a[7]);
    lds_read<28672>(a[7], la2);
    wait_vm<46>();
    asm volatile("s_barrier" ::: "memory");
    wait_lgkm<7>();
    mfma_av<false>(acc[0][0], B[8][0], a[0]);
    mfma_av<false>(acc[0][1], B[8][1], a[0]);
    mfma_av<false>(acc[0][2], B[8][2], a[0]);
    lds_read<9216>(a[0], la0);
    gload_v<3072>(B[18][1], vo, bbase + 53248);
    wait_lgkm<7>();
    mfma_av<false>(acc[1][0], B[8][0], a[1]);
    mfma_av<false>(acc[1][1], B[8][1], a[1]);
    mfma_av<false>(acc[1][2], B[8][2], a[1]);
    lds_read<29696>(a[1], la0);
    gload_v<-4096>(B[18][2], vo, bbase + 61440);
    wait_lgkm<7>();
    mfma_av<false>(acc[2][0], B[8][0], a[2]);
    mfma_av<false>(acc[2][1], B[8][1], a[2]);
    mfma_av<false>(acc[2][2], B[8][2], a[2]);
    lds_read<50176>(a[2], la0);
    gload_v<-3072>(B[19][0], vo, bbase + 61440);
    wait_lgkm<7>();
    mfma_av<false>(acc[3][0], B[8][0], a[3]);
    mfma_av<false>(acc[3][1], B[8][1], a[3]);
    mfma_av<false>(acc[3][2], B[8][2], a[3]);
    lds_read<9216>(a[3], la1);
    gload_v<-2048>(B[19][1], vo, bbase + 61440);
    wait_lgkm<7>();
    mfma_av<false>(acc[4][0], B[8][0], a[4]);
    mfma_av<false>(acc[4][1], B[8][1], a[4]);
    mfma_av<false>(acc[4][2], B[8][2], a[4]);
    lds_read<29696>(a[4], la1);
    gload_v<-1024>(B[19][2], vo, bbase + 61440);
    wait_lgkm<7>();
    mfma_av<false>(acc[5][0], B[8][0], a[5]);
    mfma_av<false>(acc[5][1], B[8][1], a[5]);
    mfma_av<false>(acc[5][2], B[8][2], a[5]);
    lds_read<50176>(a[5], la1);
    gload_a<-4096>(wd[0][0], vo, wbase + 0);
    wait_lgkm<7>();
    mfma_av<false>(acc[6][0], B[8][0], a[6]);
    mfma_av<false>(acc[6][1], B[8][1], a[6]);
    mfma_av<false>(acc[6][2], B[8][2], a[6]);
    lds_read<9216>(a[6], la2);
    wait_lgkm<7>();
    mfma_av<false>(acc[7][0], B[8][0], a[7]);
    mfma_av<false>(acc[7][1], B[8][1], a[7]);
    mfma_av<false>(acc[7][2], B[8][2], a[7]);
    lds_read<29696>(a[7], la2);
    wait_vm<49>();
    wait_lgkm<7>();
    mfma_av<false>(acc[0][0], B[9][0], a[0]);
    mfma_av<false>(acc[0][1], B[9][1], a[0]);
    mfma_av<false>(acc[0][2], B[9][2], a[0]);
    lds_read<10240>(a[0], la0);
    gload_a<-4096>(wd[0][1], vo, wbase + 49152);
    wait_lgkm<7>();
    mfma_av<false>(acc[1][0], B[9][0], a[1]);
    mfma_av<false>(acc[1][1], B[9][1], a[1]);
    mfma_av<false>(acc[1][2], B[9][2], a[1]);
    lds_read<30720>(a[1], la0);
    gload_a<-4096>(wd[0][2], vo, wbase + 98304);
    wait_lgkm<7>();
    mfma_av<false>(acc[2][0], B[9][0], a[2]);
    mfma_av<false>(acc[2][1], B[9][1], a[2]);
    mfma_av<false>(acc[2][2], B[9][2], a[2]);
    lds_read<51200>(a[2], la0);
    gload_a<-4096>(wd[0][3], vo, wbase + 147456);
    wait_lgkm<7>();
    mfma_av<false>(acc[3][0], B[9][0], a[3]);
    mfma_av<false>(acc[3][1], B[9][1], a[3]);
    mfma_av<false>(acc[3][2], B[9][2], a[3]);
    lds_read<10240>(a[3], la1);
    gload_a<-4096>(wd[0][4], vo, wbase + 196608);
    wait_lgkm<7>();
    mfma_av<false>(acc[4][0], B[9][0], a[4]);
    mfma_av<false>(acc[4][1], B[9][1], a[4]);
    mfma_av<false>(acc[4][2], B[9][2], a[4]);
    lds_read<30720>(a[4], la1);
    gload_a<-3072>(wd[1][0], vo, wbase + 0);
    wait_lgkm<7>();
    mfma_av<false>(acc[5][0], B[9][0], a[5]);
    mfma_av<false>(acc[5][1], B[9][1], a[5]);
    mfma_av<false>(acc[5][2], B[9][2], a[5]);
    lds_read<51200>(a[5], la1);
    gload_a<-3072>(wd[1][1], vo, wbase + 49152);
    wait_lgkm<7>();
    mfma_av<false>(acc[6][0], B[9][0], a[6]);
    mfma_av<false>(acc[6][1], B[9][1], a[6]);
    mfma_av<false>(acc[6][2], B[9][2], a[6]);
    lds_read<10240>(a[6], la2);
    wait_lgkm<7>();
    mfma_av<false>(acc[7][0], B[9][0], a[7]);
    mfma_av<false>(acc[7][1], B[9][1], a[7]);
    mfma_av<false>(acc[7][2], B[9][2], a[7]);
    lds_read<30720>(a[7], la2);
    wait_vm<48>();
    asm volatile("s_barrier" ::: "memory");
    wait_lgkm<7>();
    mfma_av<false>(acc[0][0], B[10][0], a[0]);
    mfma_av<false>(acc[0][1], B[10][1], a[0]);
    mfma_av<false>(acc[0][2], B[10][2], a[0]);
    lds_read<11264>(a[0], la0);
    gload_a<-3072>(wd[1][2], vo, wbase + 98304);
    wait_lgkm<7>();
    mfma_av<false>(acc[1][0], B[10][0], a[1]);
    mfma_av<false>(acc[1][1], B[10][1], a[1]);
    mfma_av<false>(acc[1][2], B[10][2], a[1]);
    lds_read<31744>(a[1], la0);
    gload_a<-3072>(wd[1][3], vo, wbase + 147456);
    wait_lgkm<7>();
    mfma_av<false>(acc[2][0], B[10][0], a[2]);
    mfma_av<false>(acc[2][1], B[10][1], a[2]);
    mfma_av<false>(acc[2][2], B[10][2], a[2]);
    lds_read<52224>(a[2], la0);
    gload_a<-3072>(wd[1][4], vo, wbase + 196608);
    wait_lgkm<7>();
    mfma_av<false>(acc[3][0], B[10][0], a[3]);
    mfma_av<false>(acc[3][1], B[10][1], a[3]);
    mfma_av<false>(acc[3][2], B[10][2], a[3]);
    lds_read<11264>(a[3], la1);
    gload_a<-2048>(wd[2][0], vo, wbase + 0);
    wait_lgkm<7>();
    mfma_av<false>(acc[4][0], B[10][0], a[4]);
    mfma_av<false>(acc[4][1], B[10][1], a[4]);
    mfma_av<false>(acc[4][2], B[10][2], a[4]);
    lds_read<31744>(a[4], la1);
    gload_a<-2048>(wd[2][1], vo, wbase + 49152);
    wait_lgkm<7>();
    mfma_av<false>(acc[5][0], B[10][0], a[5]);
    mfma_av<false>(acc[5][1], B[10][1], a[5]);
    mfma_av<false>(acc[5][2], B[10][2], a[5]);
    lds_read<52224>(a[5], la1);
    gload_a<-2048>(wd[2][2], vo, wbase + 98304);
    wait_lgkm<7>();
    mfma_av<false>(acc[6][0], B[10][0], a[6]);
    mfma_av<false>(acc[6][1], B[10][1], a[6]);
    mfma_av<false>(acc[6][2], B[10][2], a[6]);
    lds_read<11264>(a[6], la2);
    wait_lgkm<7>();
    mfma_av<false>(acc[7][0], B[10][0], a[7]);
    mfma_av<false>(acc[7][1], B[10][1], a[7]);
    mfma_av<false>(acc[7][2], B[10][2], a[7]);
    lds_read<31744>(a[7], la2);
    wait_vm<51>();
    wait_lgkm<7>();
    mfma_av<false>(acc[0][0], B[11][0], a[0]);
    mfma_av<false>(acc[0][1], B[11][1], a[0]);
    mfma_av<false>(acc[0][2], B[11][2], a[0]);
    lds_read<12288>(a[0], la0);
    gload_a<-2048>(wd[2][3], vo, wbase + 147456);
    wait_lgkm<7>();
    mfma_av<false>(acc[1][0], B[11][0], a[1]);
    mfma_av<false>(acc[1][1], B[11][1], a[1]);
    mfma_av<false>(acc[1][2], B[11][2], a[1]);
    lds_read<32768>(a[1], la0);
    gload_a<-2048>(wd[2][4], vo, wbase + 196608);
    wait_lgkm<7>();
    mfma_av<false>(acc[2][0], B[11][0], a[2]);
    mfma_av<false>(acc[2][1], B[11][1], a[2]);
    mfma_av<false>(acc[2][2], B[11][2], a[2]);
    lds_read<53248>(a[2], la0);
    gload_a<-1024>(wd[3][0], vo, wbase + 0);
    wait_lgkm<7>();
    mfma_av<false>(acc[3][0], B[11][0], a[3]);
    mfma_av<false>(acc[3][1], B[11][1], a[3]);
    mfma_av<false>(acc[3][2], B[11][2], a[3]);
    lds_read<12288>(a[3], la1);
    gload_a<-1024>(wd[3][1], vo, wbase + 49152);
    wait_lgkm<7>();
    mfma_av<false>(acc[4][0], B[11][0], a[4]);
    mfma_av<false>(acc[4][1], B[11][1], a[4]);
    mfma_av<false>(acc[4][2], B[11][2], a[4]);
    lds_read<32768>(a[4], la1);
    gload_a<-1024>(wd[3][2], vo, wbase + 98304);
    wait_lgkm<7>();
    mfma_av<false>(acc[5][0], B[11][0], a[5]);
    mfma_av<false>(acc[5][1], B[11][1], a[5]);
    mfma_av<false>(acc[5][2], B[11][2], a[5]);
    lds_read<53248>(a[5], la1);
    gload_a<-1024>(wd[3][3], vo, wbase + 147456);
    wait_lgkm<7>();
    mfma_av<false>(acc[6][0], B[11][0], a[6]);
    mfma_av<false>(acc[6][1], B[11][1], a[6]);
    mfma_av<false>(acc[6][2], B[11][2], a[6]);
    lds_read<12288>(a[6], la2);
    wait_lgkm<7>();
    mfma_av<false>(acc[7][0], B[11][0], a[7]);
    mfma_av<false>(acc[7][1], B[11][1], a[7]);
    mfma_av<false>(acc[7][2], B[11][2], a[7]);
    lds_read<32768>(a[7], la2);
    wait_vm<40>();
    asm volatile("s_barrier" ::: "memory");
    wait_lgkm<7>();
    mfma_av<false>(acc[0][0], B[12][0], a[0]);
    mfma_av<false>(acc[0][1], B[12][1], a[0]);
    mfma_av<false>(acc[0][2], B[12][2], a[0]);
    lds_read<13312>(at[0][0], la0);
    gload_a<-1024>(wd[3][4], vo, wbase + 196608);
    wait_lgkm<7>();
    mfma_av<false>(acc[1][0], B[12][0], a[1]);
    mfma_av<false>(acc[1][1], B[12][1], a[1]);
    mfma_av<false>(acc[1][2], B[12][2], a[1]);
    lds_read<14336>(at[0][1], la0);
    gload_a<0>(wd[4][0], vo, wbase + 0);
    wait_lgkm<7>();
    mfma_av<false>(acc[2][0], B[12][0], a[2]);
    mfma_av<false>(acc[2][1], B[12][1], a[2]);
    mfma_av<false>(acc[2][2], B[12][2], a[2]);
    lds_read<15360>(at[0][2], la0);
    gload_a<0>(wd[4][1], vo, wbase + 49152);
    wait_lgkm<7>();
    mfma_av<false>(acc[3][0], B[12][0], a[3]);
    mfma_av<false>(acc[3][1], B[12][1], a[3]);
    mfma_av<false>(acc[3][2], B[12][2], a[3]);
    lds_read<16384>(at[0][3], la0);
    gload_a<0>(wd[4][2], vo, wbase + 98304);
    wait_lgkm<7>();
    mfma_av<false>(acc[4][0], B[12][0], a[4]);
    mfma_av<false>(acc[4][1], B[12][1], a[4]);
    mfma_av<false>(acc[4][2], B[12][2], a[4]);
    lds_read<17408>(at[0][4], la0);
    gload_a<0>(wd[4][3], vo, wbase + 147456);
    wait_lgkm<7>();
    mfma_av<false>(acc[5][0], B[12][0], a[5]);
    mfma_av<false>(acc[5][1], B[12][1], a[5]);
    mfma_av<false>(acc[5][2], B[12][2], a[5]);
    lds_read<18432>(at[0][5], la0);
    gload_a<0>(wd[4][4], vo, wbase + 196608);
    wait_lgkm<7>();
    mfma_av<false>(acc[6][0], B[12][0], a[6]);
    mfma_av<false>(acc[6][1], B[12][1], a[6]);
    mfma_av<false>(acc[6][2], B[12][2], a[6]);
    lds_read<19456>(at[0][6], la0);
    wait_lgkm<7>();
    mfma_av<false>(acc[7][0], B[12][0], a[7]);
    mfma_av<false>(acc[7][1], B[12][1], a[7]);
    mfma_av<false>(acc[7][2], B[12][2], a[7]);
    STAMP(1);
    wait_vm<43>();
    wait_lgkm<6>();
    mfma_av<false>(acc[0][0], B[13][0], at[0][0]);
    SB();
    mfma_av<false>(acc[0][1], B[13][1], at[0][0]);
    SB();
    mfma_av<false>(acc[0][2], B[13][2], at[0][0]);
    SB();
    lds_read<33792>(at[1][0], la0);
    gload_a<1024>(wd[5][0], vo, wbase + 0);
    wait_vm<41>();
    wait_lgkm<6>();
    mfma_vv<false>(acc[0][0], B[14][0], at[0][1]);
    SB();
    mfma_vv<false>(acc[0][1], B[14][1], at[0][1]);
    SB();
    mfma_vv<false>(acc[0][2], B[14][2], at[0][1]);
    SB();
    lds_read<34816>(at[1][1], la0);
    gload2<0>(h[0], ou[0], Hin);
    wait_vm<39>();
    wait_lgkm<6>();
    mfma_vv<false>(acc[0][0], B[15][0], at[0][2]);
    SB();
    mfma_vv<false>(acc[0][1], B[15][1], at[0][2]);
    SB();
    mfma_vv<false>(acc[0][2], B[15][2], at[0][2]);
    SB();
    lds_read<35840>(at[1][2], la0);
    gload_a<1024>(wd[5][1], vo, wbase + 49152);
    wait_vm<37>();
    wait_lgkm<6>();
    mfma_vv<false>(acc[0][0], B[16][0], at[0][3]);
    SB();
    mfma_vv<false>(acc[0][1], B[16][1], at[0][3]);
    SB();
    mfma_vv<false>(acc[0][2], B[16][2], at[0][3]);
    SB();
    lds_read<36864>(at[1][3], la0);
    gload_a<1024>(wd[5][2], vo, wbase + 98304);
    wait_vm<35>();
    wait_lgkm<6>();
    mfma_vv<false>(acc[0][0], B[17][0], at[0][4]);
    SB();
    mfma_vv<false>(acc[0][1], B[17][1], at[0][4]);
    SB();
    mfma_vv<false>(acc[0][2], B[17][2], at[0][4]);
    SB();
    lds_read<37888>(at[1][4], la0);
    gload_a<1024>(wd[5][3], vo, wbase + 147456);
    wait_vm<33>();
    wait_lgkm<5>();
    mfma_vv<false>(acc[0][0], B[18][0], at[0][5]);
    SB();
    mfma_vv<false>(acc[0][1], B[18][1], at[0][5]);
    SB();
    mfma_vv<false>(acc[0][2], B[18][2], at[0][5]);
    SB();
    lds_read<38912>(at[1][5], la0);
    gload2<0>(h[1], ou[1], Hin);
    wait_vm<31>();
    mfma_vv<false>(acc[0][0], B[19][0], at[0][6]);
    SB();
    mfma_vv<false>(acc[0][1], B[19][1], at[0][6]);
    SB();
    mfma_vv<false>(acc[0][2], B[19][2], at[0][6]);
    SB();
    lds_read<39936>(at[1][6], la0);
    gload_a<1024>(wd[5][4], vo, wbase + 196608);
    wait_lgkm<6>();
    mfma_av<false>(acc[1][0], B[13][0], at[1][0]);
    SB();
    mfma_av<false>(acc[1][1], B[13][1], at[1][0]);
    SB();
    mfma_av<false>(acc[1][2], B[13][2], at[1][0]);
    SB();
    v_mul(gt[0][0], acc[0][0][0], acc[0][0][0]);
    v_mul(gt[0][1], acc[0][0][1], acc[0][0][1]);
    v_mul(gt[1][0], acc[0][1][0], acc[0][1][0]);
    v_mul(gt[1][1], acc[0][1][1], acc[0][1][1]);
    v_mul(gt[2][0], acc[0][2][0], acc[0][2][0]);
    v_mul(gt[2][1], acc[0][2][1], acc[0][2][1]);
    v_fmas(gt[0][0], gc2, gc1);
    v_fmas(gt[0][1], gc2, gc1);
    v_fmas(gt[1][0], gc2, gc1);
    lds_read<54272>(at[0][0], la0);
    gload_a<2048>(wd[6][0], vo, wbase + 0);
    wait_lgkm<5>();
    mfma_vv<false>(acc[1][0], B[14][0], at[1][1]);
    SB();
    v_fmas(gt[1][1], gc2, gc1);
    v_fmas(gt[2][0], gc2, gc1);
    v_fmas(gt[2][1], gc2, gc1);
    v_mul2(gt[0][0], acc[0][0][0]);
    v_mul2(gt[0][1], acc[0][0][1]);
    mfma_vv<false>(acc[1][1], B[14][1], at[1][1]);
    SB();
    v_mul2(gt[1][0], acc[0][1][0]);
    v_mul2(gt[1][1], acc[0][1][1]);
    v_mul2(gt[2][0], acc[0][2][0]);
    v_mul2(gt[2][1], acc[0][2][1]);
    mfma_vv<false>(acc[1][2], B[14][2], at[1][1]);
    SB();
    v_exp(gt[0][0]);
    v_mul(gu[0][0], acc[0][0][0], acc[0][0][2]);
    v_exp(gt[0][1]);
    lds_read<55296>(at[0][1], la0);
    gload_a<2048>(wd[6][1], vo, wbase + 49152);
    mfma_vv<false>(acc[1][0], B[15][0], at[1][2]);
    SB();
    v_mul(gu[0][1], acc[0][0][1], acc[0][0][3]);
    v_exp(gt[1][0]);
    v_mul(gu[1][0], acc[0][1][0], acc[0][1][2]);
    mfma_vv<false>(acc[1][1], B[15][1], at[1][2]);
    SB();
    v_exp(gt[1][1]);
    v_mul(gu[1][1], acc[0][1][1], acc[0][1][3]);
    v_exp(gt[2][0]);
    mfma_vv<false>(acc[1][2], B[15][2], at[1][2]);
    SB();
    v_mul(gu[2][0], acc[0][2][0], acc[0][2][2]);
    v_exp(gt[2][1]);
    v_mul(gu[2][1], acc[0][2][1], acc[0][2][3]);
    lds_read<56320>(at[0][2], la0);
    gload2<0>(h[2], ou[2], Hin);
    wait_lgkm<3>();
    mfma_vv<false>(acc[1][0], B[16][0], at[1][3]);
    SB();
    v_add1(gt[0][0]);
    v_add1(gt[0][1]);
    v_add1(gt[1][0]);
    v_add1(gt[1][1]);
    v_add1(gt[2][0]);
    mfma_vv<false>(acc[1][1], B[16][1], at[1][3]);
    SB();
    v_add1(gt[2][1]);
    v_rcp(gt[0][0]);
    mfma_vv<false>(acc[1][2], B[16][2], at[1][3]);
    SB();
    v_rcp(gt[0][1]);
    v_rcp(gt[1][0]);
    v_rcp(gt[1][1]);
    lds_read<57344>(at[0][3], la0);
    gload_a<2048>(wd[6][2], vo, wbase + 98304);
    mfma_vv<false>(acc[1][0], B[17][0], at[1][4]);
    SB();
    v_rcp(gt[2][0]);
    v_rcp(gt[2][1]);
    mfma_vv<false>(acc[1][1], B[17][1], at[1][4]);
    SB();
    v_mul2(gu[0][0], gt[0][0]);
    v_mul2(gu[0][1], gt[0][1]);
    v_mul2(gu[1][0], gt[1][0]);
    v_mul2(gu[1][1], gt[1][1]);
    v_mul2(gu[2][0], gt[2][0]);
    mfma_vv<false>(acc[1][2], B[17][2], at[1][4]);
    SB();
    v_mul2(gu[2][1], gt[2][1]);
    v_cvt(gp[0], gu[0][0], gu[0][1]);
    lds_read<58368>(at[0][4], la0);
    gload_a<2048>(wd[6][3], vo, wbase + 147456);
    mfma_vv<false>(acc[1][0], B[18][0], at[1][5]);
    SB();
    v_cvt(gp[1], gu[1][0], gu[1][1]);
    v_cvt(gp[2], gu[2][0], gu[2][1]);
    lds_write<0>(lw0, gp[0]);
    lds_write<1024>(lw0, gp[1]);
    mfma_vv<false>(acc[1][1], B[18][1], at[1][5]);
    SB();
    lds_write<2048>(lw0, gp[2]);
    mfma_vv<false>(acc[1][2], B[18][2], at[1][5]);
    SB();
    lds_read<59392>(at[0][5], la0);
    gload_a<2048>(wd[6][4], vo, wbase + 196608);
    mfma_vv<false>(acc[1][0], B[19][0], at[1][6]);
    SB();
    mfma_vv<false>(acc[1][1], B[19][1], at[1][6]);
    SB();
    mfma_vv<false>(acc[1][2], B[19][2], at[1][6]);
    SB();
    lds_read<60416>(at[0][6], la0);
    gload2<0>(h[3], ou[3], Hin);
    wait_lgkm<7>();
    mfma_av<false>(acc[2][0], B[13][0], at[0][0]);
    SB();
    mfma_av<false>(acc[2][1], B[13][1], at[0][0]);
    SB();
    mfma_av<false>(acc[2][2], B[13][2], at[0][0]);
    SB();
    v_mul(gt[0][0], acc[1][0][0], acc[1][0][0]);
    v_mul(gt[0][1], acc[1][0][1], acc[1][0][1]);
    v_mul(gt[1][0], acc[1][1][0], acc[1][1][0]);
    v_mul(gt[1][1], acc[1][1][1], acc[1][1][1]);
    v_mul(gt[2][0], acc[1][2][0], acc[1][2][0]);
    v_mul(gt[2][1], acc[1][2][1], acc[1][2][1]);
    v_fmas(gt[0][0], gc2, gc1);
    v_fmas(gt[0][1], gc2, gc1);
    v_fmas(gt[1][0], gc2, gc1);
    lds_read<13312>(at[1][0], la1);
    gload_a<3072>(wd[7][0], vo, wbase + 0);
    mfma_vv<false>(acc[2][0], B[14][0], at[0][1]);
    SB();
    v_fmas(gt[1][1], gc2, gc1);
    v_fmas(gt[2][0], gc2, gc1);
    v_fmas(gt[2][1], gc2, gc1);
    v_mul2(gt[0][0], acc[1][0][0]);
    v_mul2(gt[0][1], acc[1][0][1]);
    mfma_vv<false>(acc[2][1], B[14][1], at[0][1]);
    SB();
    v_mul2(gt[1][0], acc[1][1][0]);
    v_mul2(gt[1][1], acc[1][1][1]);
    v_mul2(gt[2][0], acc[1][2][0]);
    v_mul2(gt[2][1], acc[1][2][1]);
    mfma_vv<false>(acc[2][2], B[14][2], at[0][1]);
    SB();
    v_exp(gt[0][0]);
    v_mul(gu[0][0], acc[1][0][0], acc[1][0][2]);
    v_exp(gt[0][1]);
    lds_read<14336>(at[1][1], la1);
    gload_a<3072>(wd[7][1], vo, wbase + 49152);
    mfma_vv<false>(acc[2][0], B[15][0], at[0][2]);
    SB();
    v_mul(gu[0][1], acc[1][0][1], acc[1][0][3]);
    v_exp(gt[1][0]);
    v_mul(gu[1][0], acc[1][1][0], acc[1][1][2]);
    mfma_vv<false>(acc[2][1], B[15][1], at[0][2]);
    SB();
    v_exp(gt[1][1]);
    v_mul(gu[1][1], acc[1][1][1], acc[1][1][3]);
    v_exp(gt[2][0]);
    mfma_vv<false>(acc[2][2], B[15][2], at[0][2]);
    SB();
    v_mul(gu[2][0], acc[1][2][0], acc[1][2][2]);
    v_exp(gt[2][1]);
    v_mul(gu[2][1], acc[1][2][1], acc[1][2][3]);
    lds_read<15360>(at[1][2], la1);
    gload_a<3072>(wd[7][2], vo, wbase + 98304);
    wait_lgkm<3>();
    mfma_vv<false>(acc[2][0], B[16][0], at[0][3]);
    SB();
    v_add1(gt[0][0]);
    v_add1(gt[0][1]);
    v_add1(gt[1][0]);
    v_add1(gt[1][1]);
    v_add1(gt[2][0]);
    mfma_vv<false>(acc[2][1], B[16][1], at[0][3]);
    SB();
    v_add1(gt[2][1]);
    v_rcp(gt[0][0]);
    mfma_vv<false>(acc[2][2], B[16][2], at[0][3]);
    SB();
    v_rcp(gt[0][1]);
    v_rcp(gt[1][0]);
    v_rcp(gt[1][1]);
    lds_read<16384>(at[1][3], la1);
    gload2<0>(h[4], ou[4], Hin);
    mfma_vv<false>(acc[2][0], B[17][0], at[0][4]);
    SB();
    v_rcp(gt[2][0]);
    v_rcp(gt[2][1]);
    mfma_vv<false>(acc[2][1], B[17][1], at[0][4]);
    SB();
    v_mul2(gu[0][0], gt[0][0]);
    v_mul2(gu[0][1], gt[0][1]);
    v_mul2(gu[1][0], gt[1][0]);
    v_mul2(gu[1][1], gt[1][1]);
    v_mul2(gu[2][0], gt[2][0]);
    mfma_vv<false>(acc[2][2], B[17][2], at[0][4]);
    SB();
    v_mul2(gu[2][1], gt[2][1]);
    v_cvt(gp[0], gu[0][0], gu[0][1]);
    lds_read<17408>(at[1][4], la1);
    gload_a<3072>(wd[7][3], vo, wbase + 147456);
    mfma_vv<false>(acc[2][0], B[18][0], at[0][5]);
    SB();
    v_cvt(gp[1], gu[1][0], gu[1][1]);
    v_cvt(gp[2], gu[2][0], gu[2][1]);
    lds_write<20480>(lw0, gp[0]);
    lds_write<21504>(lw0, gp[1]);
    mfma_vv<false>(acc[2][1], B[18][1], at[0][5]);
    SB();
    lds_write<22528>(lw0, gp[2]);
    mfma_vv<false>(acc[2][2], B[18][2], at[0][5]);
    SB();
    lds_read<18432>(at[1][5], la1);
    wait_lgkm<1>();
    asm volatile("s_barrier" ::: "memory");
    lds_read<0>(st[0], ls0a);
    lds_read<0>(st[1], ls1a);
    gload_a<3072>(wd[7][4], vo, wbase + 196608);
    mfma_vv<false>(acc[2][0], B[19][0], at[0][6]);
    SB();
    mfma_vv<false>(acc[2][1], B[19][1], at[0][6]);
    SB();
    mfma_vv<false>(acc[2][2], B[19][2], at[0][6]);
    SB();
    lds_read<19456>(at[1][6], la1);
    wait_lgkm<2>();
    gstore4c<0>(so0, st[0], act + 0);
    mfma_av<false>(acc[3][0], B[13][0], at[1][0]);
    SB();
    mfma_av<false>(acc[3][1], B[13][1], at[1][0]);
    SB();
    v_mul(gt[0][0], acc[2][0][0], acc[2][0][0]);
    v_mul(gt[0][1], acc[2][0][1], acc[2][0][1]);
    v_mul(gt[1][0], acc[2][1][0], acc[2][1][0]);
    v_mul(gt[1][1], acc[2][1][1], acc[2][1][1]);
    v_mul(gt[2][0], acc[2][2][0], acc[2][2][0]);
    v_mul(gt[2][1], acc[2][2][1], acc[2][2][1]);
    v_fmas(gt[0][0], gc2, gc1);
    v_fmas(gt[0][1], gc2, gc1);
    v_fmas(gt[1][0], gc2, gc1);
    mfma_av<false>(acc[3][2], B[13][2], at[1][0]);
    SB();
    v_fmas(gt[1][1], gc2, gc1);
    v_fmas(gt[2][0], gc2, gc1);
    v_fmas(gt[2][1], gc2, gc1);
    v_mul2(gt[0][0], acc[2][0][0]);
    v_mul2(gt[0][1], acc[2][0][1]);
    lds_read<33792>(at[0][0], la1);
    wait_lgkm<2>();
    gstore4h(so1, st[1], act + 0);
    mfma_vv<false>(acc[3][0], B[14][0], at[1][1]);
    SB();
    v_mul2(gt[1][0], acc[2][1][0]);
    v_mul2(gt[1][1], acc[2][1][1]);
    v_mul2(gt[2][0], acc[2][2][0]);
    v_mul2(gt[2][1], acc[2][2][1]);
    mfma_vv<false>(acc[3][1], B[14][1], at[1][1]);
    SB();
    v_exp(gt[0][0]);
    v_mul(gu[0][0], acc[2][0][0], acc[2][0][2]);
    v_exp(gt[0][1]);
    mfma_vv<false>(acc[3][2], B[14][2], at[1][1]);
    SB();
    v_mul(gu[0][1], acc[2][0][1], acc[2][0][3]);
    v_exp(gt[1][0]);
    v_mul(gu[1][0], acc[2][1][0], acc[2][1][2]);
    lds_read<34816>(at[0][1], la1);
    gload_a<-4096>(wd[8][0], vo, wbase + 8192);
    mfma_vv<false>(acc[3][0], B[15][0], at[1][2]);
    SB();
    v_exp(gt[1][1]);
    v_mul(gu[1][1], acc[2][1][1], acc[2][1][3]);
    v_exp(gt[2][0]);
    mfma_vv<false>(acc[3][1], B[15][1], at[1][2]);
    SB();
    v_mul(gu[2][0], acc[2][2][0], acc[2][2][2]);
    v_exp(gt[2][1]);
    v_mul(gu[2][1], acc[2][2][1], acc[2][2][3]);
    mfma_vv<false>(acc[3][2], B[15][2], at[1][2]);
    SB();
    v_add1(gt[0][0]);
    v_add1(gt[0][1]);
    v_add1(gt[1][0]);
    v_add1(gt[1][1]);
    v_add1(gt[2][0]);
    lds_read<35840>(at[0][2], la1);
    gload2<0>(h[5], ou[5], Hin);
    mfma_vv<false>(acc[3][0], B[16][0], at[1][3]);
    SB();
    v_add1(gt[2][1]);
    v_rcp(gt[0][0]);
    mfma_vv<false>(acc[3][1], B[16][1], at[1][3]);
    SB();
    v_rcp(gt[0][1]);
    v_rcp(gt[1][0]);
    v_rcp(gt[1][1]);
    mfma_vv<false>(acc[3][2], B[16][2], at[1][3]);
    SB();
    v_rcp(gt[2][0]);
    v_rcp(gt[2][1]);
    lds_read<36864>(at[0][3], la1);
    gload_a<-4096>(wd[8][1], vo, wbase + 57344);
    mfma_vv<false>(acc[3][0], B[17][0], at[1][4]);
    SB();
    v_mul2(gu[0][0], gt[0][0]);
    v_mul2(gu[0][1], gt[0][1]);
    v_mul2(gu[1][0], gt[1][0]);
    v_mul2(gu[1][1], gt[1][1]);
    v_mul2(gu[2][0], gt[2][0]);
    mfma_vv<false>(acc[3][1], B[17][1], at[1][4]);
    SB();
    v_mul2(gu[2][1], gt[2][1]);
    v_cvt(gp[0], gu[0][0], gu[0][1]);
    mfma_vv<false>(acc[3][2], B[17][2], at[1][4]);
    SB();
    v_cvt(gp[1], gu[1][0], gu[1][1]);
    v_cvt(gp[2], gu[2][0], gu[2][1]);
    lds_write<40960>(lw0, gp[0]);
    lds_write<41984>(lw0, gp[1]);
    lds_read<37888>(at[0][4], la1);
    gload_a<-4096>(wd[8][2], vo, wbase + 106496);
    mfma_vv<false>(acc[3][0], B[18][0], at[1][5]);
    SB();
    lds_write<43008>(lw0, gp[2]);
    mfma_vv<false>(acc[3][1], B[18][1], at[1][5]);
    SB();
    mfma_vv<false>(acc[3][2], B[18][2], at[1][5]);
    SB();
    lds_read<38912>(at[0][5], la1);
    gload_a<-4096>(wd[8][3], vo, wbase + 155648);
    wait_lgkm<7>();
    mfma_vv<false>(acc[3][0], B[19][0], at[1][6]);
    SB();
    mfma_vv<false>(acc[3][1], B[19][1], at[1][6]);
    SB();
    mfma_vv<false>(acc[3][2], B[19][2], at[1][6]);
    SB();
    lds_read<39936>(at[0][6], la1);
    gload_a<-4096>(wd[8][4], vo, wbase + 204800);
    mfma_av<false>(acc[4][0], B[13][0], at[0][0]);
    SB();
    mfma_av<false>(acc[4][1], B[13][1], at[0][0]);
    SB();
    mfma_av<false>(acc[4][2], B[13][2], at[0][0]);
    SB();
    lds_read<54272>(at[1][0], la1);
    gload_a<-3072>(wd[9][0], vo, wbase + 8192);
    mfma_vv<false>(acc[4][0], B[14][0], at[0][1]);
    SB();
    v_mul(gt[0][0], acc[3][0][0], acc[3][0][0]);
    v_mul(gt[0][1], acc[3][0][1], acc[3][0][1]);
    v_mul(gt[1][0], acc[3][1][0], acc[3][1][0]);
    v_mul(gt[1][1], acc[3][1][1], acc[3][1][1]);
    v_mul(gt[2][0], acc[3][2][0], acc[3][2][0]);
    v_mul(gt[2][1], acc[3][2][1], acc[3][2][1]);
    v_fmas(gt[0][0], gc2, gc1);
    v_fmas(gt[0][1], gc2, gc1);
    v_fmas(gt[1][0], gc2, gc1);
    mfma_vv<false>(acc[4][1], B[14][1], at[0][1]);
    SB();
    v_fmas(gt[1][1], gc2, gc1);
    v_fmas(gt[2][0], gc2, gc1);
    v_fmas(gt[2][1], gc2, gc1);
    v_mul2(gt[0][0], acc[3][0][0]);
    v_mul2(gt[0][1], acc[3][0][1]);
    mfma_vv<false>(acc[4][2], B[14][2], at[0][1]);
    SB();
    v_mul2(gt[1][0], acc[3][1][0]);
    v_mul2(gt[1][1], acc[3][1][1]);
    v_mul2(gt[2][0], acc[3][2][0]);
    v_mul2(gt[2][1], acc[3][2][1]);
    lds_read<55296>(at[1][1], la1);
    gload_a<-3072>(wd[9][1], vo, wbase + 57344);
    wait_lgkm<5>();
    mfma_vv<false>(acc[4][0], B[15][0], at[0][2]);
    SB();
    v_exp(gt[0][0]);
    v_mul(gu[0][0], acc[3][0][0], acc[3][0][2]);
    v_exp(gt[0][1]);
    mfma_vv<false>(acc[4][1], B[15][1], at[0][2]);
    SB();
    v_mul(gu[0][1], acc[3][0][1], acc[3][0][3]);
    v_exp(gt[1][0]);
    v_mul(gu[1][0], acc[3][1][0], acc[3][1][2]);
    mfma_vv<false>(acc[4][2], B[15][2], at[0][2]);
    SB();
    v_exp(gt[1][1]);
    v_mul(gu[1][1], acc[3][1][1], acc[3][1][3]);
    v_exp(gt[2][0]);
    lds_read<56320>(at[1][2], la1);
    gload_a<-3072>(wd[9][2], vo, wbase + 106496);
    mfma_vv<false>(acc[4][0], B[16][0], at[0][3]);
    SB();
    v_mul(gu[2][0], acc[3][2][0], acc[3][2][2]);
    v_exp(gt[2][1]);
    v_mul(gu[2][1], acc[3][2][1], acc[3][2][3]);
    mfma_vv<false>(acc[4][1], B[16][1], at[0][3]);
    SB();
    v_add1(gt[0][0]);
    v_add1(gt[0][1]);
    v_add1(gt[1][0]);
    v_add1(gt[1][1]);
    v_add1(gt[2][0]);
    mfma_vv<false>(acc[4][2], B[16][2], at[0][3]);
    SB();
    v_add1(gt[2][1]);
    v_rcp(gt[0][0]);
    lds_read<57344>(at[1][3], la1);
    gload_a<-3072>(wd[9][3], vo, wbase + 155648);
    mfma_vv<false>(acc[4][0], B[17][0], at[0][4]);
    SB();
    v_rcp(gt[0][1]);
    v_rcp(gt[1][0]);
    v_rcp(gt[1][1]);
    mfma_vv<false>(acc[4][1], B[17][1], at[0][4]);
    SB();
    v_rcp(gt[2][0]);
    v_rcp(gt[2][1]);
    mfma_vv<false>(acc[4][2], B[17][2], at[0][4]);
    SB();
    v_mul2(gu[0][0], gt[0][0]);
    v_mul2(gu[0][1], gt[0][1]);
    v_mul2(gu[1][0], gt[1][0]);
    v_mul2(gu[1][1], gt[1][1]);
    v_mul2(gu[2][0], gt[2][0]);
    lds_read<58368>(at[1][4], la1);
    gload_a<-3072>(wd[9][4], vo, wbase + 204800);
    wait_lgkm<3>();
    mfma_vv<false>(acc[4][0], B[18][0], at[0][5]);
    SB();
    v_mul2(gu[2][1], gt[2][1]);
    v_cvt(gp[0], gu[0][0], gu[0][1]);
    mfma_vv<false>(acc[4][1], B[18][1], at[0][5]);
    SB();
    v_cvt(gp[1], gu[1][0], gu[1][1]);
    v_cvt(gp[2], gu[2][0], gu[2][1]);
    lds_write<0>(lw1, gp[0]);
    lds_write<1024>(lw1, gp[1]);
    mfma_vv<false>(acc[4][2], B[18][2], at[0][5]);
    SB();
    lds_write<2048>(lw1, gp[2]);
    lds_read<59392>(at[1][5], la1);
    wait_lgkm<1>();
    asm volatile("s_barrier" ::: "memory");
    lds_read<40960>(st[0], ls0a);
    lds_read<40960>(st[1], ls1a);
    gload_a<-2048>(wd[10][0], vo, wbase + 8192);
    mfma_vv<false>(acc[4][0], B[19][0], at[0][6]);
    SB();
    mfma_vv<false>(acc[4][1], B[19][1], at[0][6]);
    SB();
    mfma_vv<false>(acc[4][2], B[19][2], at[0][6]);
    SB();
    lds_read<60416>(at[1][6], la1);
    wait_lgkm<2>();
    gstore4c<0>(so0, st[0], act + 98304);
    mfma_av<false>(acc[5][0], B[13][0], at[1][0]);
    SB();
    mfma_av<false>(acc[5][1], B[13][1], at[1][0]);
    SB();
    v_mul(gt[0][0], acc[4][0][0], acc[4][0][0]);
    v_mul(gt[0][1], acc[4][0][1], acc[4][0][1]);
    v_mul(gt[1][0], acc[4][1][0], acc[4][1][0]);
    v_mul(gt[1][1], acc[4][1][1], acc[4][1][1]);
    v_mul(gt[2][0], acc[4][2][0], acc[4][2][0]);
    v_mul(gt[2][1], acc[4][2][1], acc[4][2][1]);
    v_fmas(gt[0][0], gc2, gc1);
    v_fmas(gt[0][1], gc2, gc1);
    v_fmas(gt[1][0], gc2, gc1);
    mfma_av<false>(acc[5][2], B[13][2], at[1][0]);
    SB();
    v_fmas(gt[1][1], gc2, gc1);
    v_fmas(gt[2][0], gc2, gc1);
    v_fmas(gt[2][1], gc2, gc1);
    v_mul2(gt[0][0], acc[4][0][0]);
    v_mul2(gt[0][1], acc[4][0][1]);
    lds_read<13312>(at[0][0], la2);
    wait_lgkm<2>();
    gstore4h(so1, st[1], act + 98304);
    mfma_vv<false>(acc[5][0], B[14][0], at[1][1]);
    SB();
    v_mul2(gt[1][0], acc[4][1][0]);
    v_mul2(gt[1][1], acc[4][1][1]);
    v_mul2(gt[2][0], acc[4][2][0]);
    v_mul2(gt[2][1], acc[4][2][1]);
    mfma_vv<false>(acc[5][1], B[14][1], at[1][1]);
    SB();
    v_exp(gt[0][0]);
    v_mul(gu[0][0], acc[4][0][0], acc[4][0][2]);
    v_exp(gt[0][1]);
    mfma_vv<false>(acc[5][2], B[14][2], at[1][1]);
    SB();
    v_mul(gu[0][1], acc[4][0][1], acc[4][0][3]);
    v_exp(gt[1][0]);
    v_mul(gu[1][0], acc[4][1][0], acc[4][1][2]);
    lds_read<14336>(at[0][1], la2);
    gload_a<-2048>(wd[10][1], vo, wbase + 57344);
    mfma_vv<false>(acc[5][0], B[15][0], at[1][2]);
    SB();
    v_exp(gt[1][1]);
    v_mul(gu[1][1], acc[4][1][1], acc[4][1][3]);
    v_exp(gt[2][0]);
    mfma_vv<false>(acc[5][1], B[15][1], at[1][2]);
    SB();
    v_mul(gu[2][0], acc[4][2][0], acc[4][2][2]);
    v_exp(gt[2][1]);
    v_mul(gu[2][1], acc[4][2][1], acc[4][2][3]);
    mfma_vv<false>(acc[5][2], B[15][2], at[1][2]);
    SB();
    v_add1(gt[0][0]);
    v_add1(gt[0][1]);
    v_add1(gt[1][0]);
    v_add1(gt[1][1]);
    v_add1(gt[2][0]);
    lds_read<15360>(at[0][2], la2);
    gload_a<-2048>(wd[10][2], vo, wbase + 106496);
    mfma_vv<false>(acc[5][0], B[16][0], at[1][3]);
    SB();
    v_add1(gt[2][1]);
    v_rcp(gt[0][0]);
    mfma_vv<false>(acc[5][1], B[16][1], at[1][3]);
    SB();
    v_rcp(gt[0][1]);
    v_rcp(gt[1][0]);
    v_rcp(gt[1][1]);
    mfma_vv<false>(acc[5][2], B[16][2], at[1][3]);
    SB();
    v_rcp(gt[2][0]);
    v_rcp(gt[2][1]);
    lds_read<16384>(at[0][3], la2);
    gload_a<-2048>(wd[10][3], vo, wbase + 155648);
    mfma_vv<false>(acc[5][0], B[17][0], at[1][4]);
    SB();
    v_mul2(gu[0][0], gt[0][0]);
    v_mul2(gu[0][1], gt[0][1]);
    v_mul2(gu[1][0], gt[1][0]);
    v_mul2(gu[1][1], gt[1][1]);
    v_mul2(gu[2][0], gt[2][0]);
    mfma_vv<false>(acc[5][1], B[17][1], at[1][4]);
    SB();
    v_mul2(gu[2][1], gt[2][1]);
    v_cvt(gp[0], gu[0][0], gu[0][1]);
    mfma_vv<false>(acc[5][2], B[17][2], at[1][4]);
    SB();
    v_cvt(gp[1], gu[1][0], gu[1][1]);
    v_cvt(gp[2], gu[2][0], gu[2][1]);
    lds_write<20480>(lw1, gp[0]);
    lds_write<21504>(lw1, gp[1]);
    lds_read<17408>(at[0][4], la2);
    gload_a<-2048>(wd[10][4], vo, wbase + 204800);
    mfma_vv<false>(acc[5][0], B[18][0], at[1][5]);
    SB();
    lds_write<22528>(lw1, gp[2]);
    mfma_vv<false>(acc[5][1], B[18][1], at[1][5]);
    SB();
    mfma_vv<false>(acc[5][2], B[18][2], at[1][5]);
    SB();
    lds_read<18432>(at[0][5], la2);
    gload_a<-1024>(wd[11][0], vo, wbase + 8192);
    wait_lgkm<7>();
    mfma_vv<false>(acc[5][0], B[19][0], at[1][6]);
    SB();
    mfma_vv<false>(acc[5][1], B[19][1], at[1][6]);
    SB();
    mfma_vv<false>(acc[5][2], B[19][2], at[1][6]);
    SB();
    lds_read<19456>(at[0][6], la2);
    gload_a<-1024>(wd[11][1], vo, wbase + 57344);
    mfma_av<false>(acc[6][0], B[13][0], at[0][0]);
    SB();
    mfma_av<false>(acc[6][1], B[13][1], at[0][0]);
    SB();
    mfma_av<false>(acc[6][2], B[13][2], at[0][0]);
    SB();
    lds_read<33792>(at[1][0], la2);
    gload_a<-1024>(wd[11][2], vo, wbase + 106496);
    mfma_vv<false>(acc[6][0], B[14][0], at[0][1]);
    SB();
    v_mul(gt[0][0], acc[5][0][0], acc[5][0][0]);
    v_mul(gt[0][1], acc[5][0][1], acc[5][0][1]);
    v_mul(gt[1][0], acc[5][1][0], acc[5][1][0]);
    v_mul(gt[1][1], acc[5][1][1], acc[5][1][1]);
    v_mul(gt[2][0], acc[5][2][0], acc[5][2][0]);
    v_mul(gt[2][1], acc[5][2][1], acc[5][2][1]);
    v_fmas(gt[0][0], gc2, gc1);
    v_fmas(gt[0][1], gc2, gc1);
    v_fmas(gt[1][0], gc2, gc1);
    mfma_vv<false>(acc[6][1], B[14][1], at[0][1]);
    SB();
    v_fmas(gt[1][1], gc2, gc1);
    v_fmas(gt[2][0], gc2, gc1);
    v_fmas(gt[2][1], gc2, gc1);
    v_mul2(gt[0][0], acc[5][0][0]);
    v_mul2(gt[0][1], acc[5][0][1]);
    mfma_vv<false>(acc[6][2], B[14][2], at[0][1]);
    SB();
    v_mul2(gt[1][0], acc[5][1][0]);
    v_mul2(gt[1][1], acc[5][1][1]);
    v_mul2(gt[2][0], acc[5][2][0]);
    v_mul2(gt[2][1], acc[5][2][1]);
    lds_read<34816>(at[1][1], la2);
    gload_a<-1024>(wd[11][3], vo, wbase + 155648);
    wait_lgkm<5>();
    mfma_vv<false>(acc[6][0], B[15][0], at[0][2]);
    SB();
    v_exp(gt[0][0]);
    v_mul(gu[0][0], acc[5][0][0], acc[5][0][2]);
    v_exp(gt[0][1]);
    mfma_vv<false>(acc[6][1], B[15][1], at[0][2]);
    SB();
    v_mul(gu[0][1], acc[5][0][1], acc[5][0][3]);
    v_exp(gt[1][0]);
    v_mul(gu[1][0], acc[5][1][0], acc[5][1][2]);
    mfma_vv<false>(acc[6][2], B[15][2], at[0][2]);
    SB();
    v_exp(gt[1][1]);
    v_mul(gu[1][1], acc[5][1][1], acc[5][1][3]);
    v_exp(gt[2][0]);
    lds_read<35840>(at[1][2], la2);
    gload_vc<-4096>(E[0][0], vo, eb[0] + 0);
    mfma_vv<false>(acc[6][0], B[16][0], at[0][3]);
    SB();
    v_mul(gu[2][0], acc[5][2][0], acc[5][2][2]);
    v_exp(gt[2][1]);
    v_mul(gu[2][1], acc[5][2][1], acc[5][2][3]);
    mfma_vv<false>(acc[6][1], B[16][1], at[0][3]);
    SB();
    v_add1(gt[0][0]);
    v_add1(gt[0][1]);
    v_add1(gt[1][0]);
    v_add1(gt[1][1]);
    v_add1(gt[2][0]);
    mfma_vv<false>(acc[6][2], B[16][2], at[0][3]);
    SB();
    v_add1(gt[2][1]);
    v_rcp(gt[0][0]);
    lds_read<36864>(at[1][3], la2);
    gload_vc<-3072>(E[0][1], vo, eb[0] + 0);
    mfma_vv<false>(acc[6][0], B[17][0], at[0][4]);
    SB();
    v_rcp(gt[0][1]);
    v_rcp(gt[1][0]);
    v_rcp(gt[1][1]);
    mfma_vv<false>(acc[6][1], B[17][1], at[0][4]);
    SB();
    v_rcp(gt[2][0]);
    v_rcp(gt[2][1]);
    mfma_vv<false>(acc[6][2], B[17][2], at[0][4]);
    SB();
    v_mul2(gu[0][0], gt[0][0]);
    v_mul2(gu[0][1], gt[0][1]);
    v_mul2(gu[1][0], gt[1][0]);
    v_mul2(gu[1][1], gt[1][1]);
    v_mul2(gu[2][0], gt[2][0]);
    lds_read<37888>(at[1][4], la2);
    gload_vc<-2048>(E[0][2], vo, eb[0] + 0);
    wait_lgkm<3>();
    mfma_vv<false>(acc[6][0], B[18][0], at[0][5]);
    SB();
    v_mul2(gu[2][1], gt[2][1]);
    v_cvt(gp[0], gu[0][0], gu[0][1]);
    mfma_vv<false>(acc[6][1], B[18][1], at[0][5]);
    SB();
    v_cvt(gp[1], gu[1][0], gu[1][1]);
    v_cvt(gp[2], gu[2][0], gu[2][1]);
    lds_write<40960>(lw1, gp[0]);
    lds_write<41984>(lw1, gp[1]);
    mfma_vv<false>(acc[6][2], B[18][2], at[0][5]);
    SB();
    lds_write<43008>(lw1, gp[2]);
    lds_read<38912>(at[1][5], la2);
    wait_lgkm<1>();
    asm volatile("s_barrier" ::: "memory");
    lds_read<0>(st[0], ls0b);
    lds_read<0>(st[1], ls1b);
    gload_vc<-1024>(E[0][3], vo, eb[0] + 0);
    mfma_vv<false>(acc[6][0], B[19][0], at[0][6]);
    SB();
    mfma_vv<false>(acc[6][1], B[19][1], at[0][6]);
    SB();
    mfma_vv<false>(acc[6][2], B[19][2], at[0][6]);
    SB();
    lds_read<39936>(at[1][6], la2);
    wait_lgkm<2>();
    gstore4c<0>(so0, st[0], act + 196608);
    mfma_av<false>(acc[7][0], B[13][0], at[1][0]);
    SB();
    mfma_av<false>(acc[7][1], B[13][1], at[1][0]);
    SB();
    v_mul(gt[0][0], acc[6][0][0], acc[6][0][0]);
    v_mul(gt[0][1], acc[6][0][1], acc[6][0][1]);
    v_mul(gt[1][0], acc[6][1][0], acc[6][1][0]);
    v_mul(gt[1][1], acc[6][1][1], acc[6][1][1]);
    v_mul(gt[2][0], acc[6][2][0], acc[6][2][0]);
    v_mul(gt[2][1], acc[6][2][1], acc[6][2][1]);
    v_fmas(gt[0][0], gc2, gc1);
    v_fmas(gt[0][1], gc2, gc1);
    v_fmas(gt[1][0], gc2, gc1);
    mfma_av<false>(acc[7][2], B[13][2], at[1][0]);
    SB();
    v_fmas(gt[1][1], gc2, gc1);
    v_fmas(gt[2][0], gc2, gc1);
    v_fmas(gt[2][1], gc2, gc1);
    v_mul2(gt[0][0], acc[6][0][0]);
    v_mul2(gt[0][1], acc[6][0][1]);
    wait_lgkm<1>();
    gstore4h(so1, st[1], act + 196608);
    mfma_vv<false>(acc[7][0], B[14][0], at[1][1]);
    SB();
    v_mul2(gt[1][0], acc[6][1][0]);
    v_mul2(gt[1][1], acc[6][1][1]);
    v_mul2(gt[2][0], acc[6][2][0]);
    v_mul2(gt[2][1], acc[6][2][1]);
    mfma_vv<false>(acc[7][1], B[14][1], at[1][1]);
    SB();
    v_exp(gt[0][0]);
    v_mul(gu[0][0], acc[6][0][0], acc[6][0][2]);
    v_exp(gt[0][1]);
    mfma_vv<false>(acc[7][2], B[14][2], at[1][1]);
    SB();
    v_mul(gu[0][1], acc[6][0][1], acc[6][0][3]);
    v_exp(gt[1][0]);
    v_mul(gu[1][0], acc[6][1][0], acc[6][1][2]);
    gload_a<-1024>(wd[11][4], vo, wbase + 204800);
    mfma_vv<false>(acc[7][0], B[15][0], at[1][2]);
    SB();
    v_exp(gt[1][1]);
    v_mul(gu[1][1], acc[6][1][1], acc[6][1][3]);
    v_exp(gt[2][0]);
    mfma_vv<false>(acc[7][1], B[15][1], at[1][2]);
    SB();
    v_mul(gu[2][0], acc[6][2][0], acc[6][2][2]);
    v_exp(gt[2][1]);
    v_mul(gu[2][1], acc[6][2][1], acc[6][2][3]);
    mfma_vv<false>(acc[7][2], B[15][2], at[1][2]);
    SB();
    v_add1(gt[0][0]);
    v_add1(gt[0][1]);
    v_add1(gt[1][0]);
    v_add1(gt[1][1]);
    v_add1(gt[2][0]);
    gload_vc<0>(E[0][4], vo, eb[0] + 0);
    mfma_vv<false>(acc[7][0], B[16][0], at[1][3]);
    SB();
    v_add1(gt[2][1]);
    v_rcp(gt[0][0]);
    mfma_vv<false>(acc[7][1], B[16][1], at[1][3]);
    SB();
    v_rcp(gt[0][1]);
    v_rcp(gt[1][0]);
    v_rcp(gt[1][1]);
    mfma_vv<false>(acc[7][2], B[16][2], at[1][3]);
    SB();
    v_rcp(gt[2][0]);
    v_rcp(gt[2][1]);
    gload_vc<1024>(E[0][5], vo, eb[0] + 0);
    mfma_vv<false>(acc[7][0], B[17][0], at[1][4]);
    SB();
    v_mul2(gu[0][0], gt[0][0]);
    v_mul2(gu[0][1], gt[0][1]);
    v_mul2(gu[1][0], gt[1][0]);
    v_mul2(gu[1][1], gt[1][1]);
    v_mul2(gu[2][0], gt[2][0]);
    mfma_vv<false>(acc[7][1], B[17][1], at[1][4]);
    SB();
    v_mul2(gu[2][1], gt[2][1]);
    v_cvt(gp[0], gu[0][0], gu[0][1]);
    mfma_vv<false>(acc[7][2], B[17][2], at[1][4]);
    SB();
    v_cvt(gp[1], gu[1][0], gu[1][1]);
    v_cvt(gp[2], gu[2][0], gu[2][1]);
    lds_write<0>(lw2, gp[0]);
    lds_write<1024>(lw2, gp[1]);
    gload_vc<2048>(E[0][6], vo, eb[0] + 0);
    mfma_vv<false>(acc[7][0], B[18][0], at[1][5]);
    SB();
    lds_write<2048>(lw2, gp[2]);
    mfma_vv<false>(acc[7][1], B[18][1], at[1][5]);
    SB();
    mfma_vv<false>(acc[7][2], B[18][2], at[1][5]);
    SB();
    wait_lgkm<3>();
    mfma_vv<false>(acc[7][0], B[19][0], at[1][6]);
    SB();
    mfma_vv<false>(acc[7][1], B[19][1], at[1][6]);
    SB();
    mfma_vv<false>(acc[7][2], B[19][2], at[1][6]);
    SB();
    gload_vc<3072>(E[0][7], vo, eb[0] + 0);
    STAMP(2);
    wait_vm<10>();
    mfma_av<true>(ac2[0][0], wd[0][0], E[0][0]);
    SB();
    v_max3(mx[0], E[0][0][0], E[0][0][1]);
    mfma_av<true>(ac2[0][1], wd[0][1], E[0][0]);
    SB();
    v_max3(mx[1], E[0][0][2], E[0][0][3]);
    v_mul(gt[0][0], acc[7][0][0], acc[7][0][0]);
    v_mul(gt[0][1], acc[7][0][1], acc[7][0][1]);
    v_mul(gt[1][0], acc[7][1][0], acc[7][1][0]);
    v_mul(gt[1][1], acc[7][1][1], acc[7][1][1]);
    v_mul(gt[2][0], acc[7][2][0], acc[7][2][0]);
    v_mul(gt[2][1], acc[7][2][1], acc[7][2][1]);
    mfma_av<true>(ac2[0][2], wd[0][2], E[0][0]);
    SB();
    v_fmas(gt[0][0], gc2, gc1);
    v_fmas(gt[0][1], gc2, gc1);
    v_fmas(gt[1][0], gc2, gc1);
    gload_vc<-4096>(E[0][8], vo, eb[0] + 8192);
    mfma_av<true>(ac2[0][3], wd[0][3], E[0][0]);
    SB();
    v_fmas(gt[1][1], gc2, gc1);
    v_fmas(gt[2][0], gc2, gc1);
    v_fmas(gt[2][1], gc2, gc1);
    mfma_av<true>(ac2[0][4], wd[0][4], E[0][0]);
    SB();
    v_mul2(gt[0][0], acc[7][0][0]);
    v_mul2(gt[0][1], acc[7][0][1]);
    v_mul2(gt[1][0], acc[7][1][0]);
    gload_vc<-3072>(E[0][9], vo, eb[0] + 8192);
    wait_vm<11>();
    mfma_av<false>(ac2[0][0], wd[1][0], E[0][1]);
    SB();
    v_max3(mx[0], E[0][1][0], E[0][1][1]);
    v_mul2(gt[1][1], acc[7][1][1]);
    v_mul2(gt[2][0], acc[7][2][0]);
    mfma_av<false>(ac2[0][1], wd[1][1], E[0][1]);
    SB();
    v_max3(mx[1], E[0][1][2], E[0][1][3]);
    v_mul2(gt[2][1], acc[7][2][1]);
    mfma_av<false>(ac2[0][2], wd[1][2], E[0][1]);
    SB();
    v_exp(gt[0][0]);
    v_mul(gu[0][0], acc[7][0][0], acc[7][0][2]);
    gload_vc<-2048>(E[0][10], vo, eb[0] + 8192);
    mfma_av<false>(ac2[0][3], wd[1][3], E[0][1]);
    SB();
    v_exp(gt[0][1]);
    v_mul(gu[0][1], acc[7][0][1], acc[7][0][3]);
    mfma_av<false>(ac2[0][4], wd[1][4], E[0][1]);
    SB();
    v_exp(gt[1][0]);
    v_mul(gu[1][0], acc[7][1][0], acc[7][1][2]);
    wait_vm<11>();
    mfma_av<false>(ac2[0][0], wd[2][0], E[0][2]);
    SB();
    v_max3(mx[0], E[0][2][0], E[0][2][1]);
    v_exp(gt[1][1]);
    v_mul(gu[1][1], acc[7][1][1], acc[7][1][3]);
    mfma_av<false>(ac2[0][1], wd[2][1], E[0][2]);
    SB();
    v_max3(mx[1], E[0][2][2], E[0][2][3]);
    v_exp(gt[2][0]);
    mfma_av<false>(ac2[0][2], wd[2][2], E[0][2]);
    SB();
    v_mul(gu[2][0], acc[7][2][0], acc[7][2][2]);
    v_exp(gt[2][1]);
    gload_vc<-1024>(E[0][11], vo, eb[0] + 8192);
    mfma_av<false>(ac2[0][3], wd[2][3], E[0][2]);
    SB();
    v_mul(gu[2][1], acc[7][2][1], acc[7][2][3]);
    v_add1(gt[0][0]);
    v_add1(gt[0][1]);
    mfma_av<false>(ac2[0][4], wd[2][4], E[0][2]);
    SB();
    v_add1(gt[1][0]);
    v_add1(gt[1][1]);
    v_add1(gt[2][0]);
    gload_vc<-4096>(E[1][0], vo, eb[1] + 0);
    wait_vm<12>();
    mfma_av<false>(ac2[0][0], wd[3][0], E[0][3]);
    SB();
    v_max3(mx[0], E[0][3][0], E[0][3][1]);
    v_add1(gt[2][1]);
    mfma_av<false>(ac2[0][1], wd[3][1], E[0][3]);
    SB();
    v_max3(mx[1], E[0][3][2], E[0][3][3]);
    v_rcp(gt[0][0]);
    mfma_av<false>(ac2[0][2], wd[3][2], E[0][3]);
    SB();
    v_rcp(gt[0][1]);
    v_rcp(gt[1][0]);
    gload_vc<-3072>(E[1][1], vo, eb[1] + 0);
    mfma_av<false>(ac2[0][3], wd[3][3], E[0][3]);
    SB();
    v_rcp(gt[1][1]);
    mfma_av<false>(ac2[0][4], wd[3][4], E[0][3]);
    SB();
    v_rcp(gt[2][0]);
    v_rcp(gt[2][1]);
    wait_vm<9>();
    mfma_av<false>(ac2[0][0], wd[4][0], E[0][4]);
    SB();
    v_max3(mx[0], E[0][4][0], E[0][4][1]);
    v_mul2(gu[0][0], gt[0][0]);
    v_mul2(gu[0][1], gt[0][1]);
    mfma_av<false>(ac2[0][1], wd[4][1], E[0][4]);
    SB();
    v_max3(mx[1], E[0][4][2], E[0][4][3]);
    v_mul2(gu[1][0], gt[1][0]);
    v_mul2(gu[1][1], gt[1][1]);
    mfma_av<false>(ac2[0][2], wd[4][2], E[0][4]);
    SB();
    v_mul2(gu[2][0], gt[2][0]);
    v_mul2(gu[2][1], gt[2][1]);
    gload_vc<-2048>(E[1][2], vo, eb[1] + 0);
    mfma_av<false>(ac2[0][3], wd[4][3], E[0][4]);
    SB();
    v_cvt(gp[0], gu[0][0], gu[0][1]);
    v_cvt(gp[1], gu[1][0], gu[1][1]);
    mfma_av<false>(ac2[0][4], wd[4][4], E[0][4]);
    SB();
    v_cvt(gp[2], gu[2][0], gu[2][1]);
    lds_write<20480>(lw2, gp[0]);
    wait_vm<9>();
    mfma_av<false>(ac2[0][0], wd[5][0], E[0][5]);
    SB();
    v_max3(mx[0], E[0][5][0], E[0][5][1]);
    lds_write<21504>(lw2, gp[1]);
    lds_write<22528>(lw2, gp[2]);
    mfma_av<false>(ac2[0][1], wd[5][1], E[0][5]);
    SB();
    v_max3(mx[1], E[0][5][2], E[0][5][3]);
    mfma_av<false>(ac2[0][2], wd[5][2], E[0][5]);
    SB();
    wait_lgkm<0>();
    asm volatile("s_barrier" ::: "memory");
    lds_read<40960>(st[0], ls0b);
    lds_read<40960>(st[1], ls1b);
    gload_vc<-1024>(E[1][3], vo, eb[1] + 0);
    mfma_av<false>(ac2[0][3], wd[5][3], E[0][5]);
    SB();
    mfma_av<false>(ac2[0][4], wd[5][4], E[0][5]);
    SB();
    wait_lgkm<1>();
    gstore4c<0>(so0, st[0], act + 294912);
    wait_vm<10>();
    mfma_av<false>(ac2[0][0], wd[6][0], E[0][6]);
    SB();
    v_max3(mx[0], E[0][6][0], E[0][6][1]);
    mfma_av<false>(ac2[0][1], wd[6][1], E[0][6]);
    SB();
    v_max3(mx[1], E[0][6][2], E[0][6][3]);
    mfma_av<false>(ac2[0][2], wd[6][2], E[0][6]);
    SB();
    wait_lgkm<0>();
    gstore4h(so1, st[1], act + 294912);
    STAMP(3);
    mfma_av<false>(ac2[0][3], wd[6][3], E[0][6]);
    SB();
    mfma_av<false>(ac2[0][4], wd[6][4], E[0][6]);
    SB();
    wait_vm<10>();
    mfma_av<false>(ac2[0][0], wd[7][0], E[0][7]);
    SB();
    v_max3(mx[0], E[0][7][0], E[0][7][1]);
    mfma_av<false>(ac2[0][1], wd[7][1], E[0][7]);
    SB();
    v_max3(mx[1], E[0][7][2], E[0][7][3]);
    mfma_av<false>(ac2[0][2], wd[7][2], E[0][7]);
    SB();
    mfma_av<false>(ac2[0][3], wd[7][3], E[0][7]);
    SB();
    mfma_av<false>(ac2[0][4], wd[7][4], E[0][7]);
    SB();
    gload_vc<0>(E[1][4], vo, eb[1] + 0);
    wait_vm<10>();
    mfma_av<false>(ac2[0][0], wd[8][0], E[0][8]);
    SB();
    v_max3(mx[0], E[0][8][0], E[0][8][1]);
    mfma_av<false>(ac2[0][1], wd[8][1], E[0][8]);
    SB();
    v_max3(mx[1], E[0][8][2], E[0][8][3]);
    mfma_av<false>(ac2[0][2], wd[8][2], E[0][8]);
    SB();
    mfma_av<false>(ac2[0][3], wd[8][3], E[0][8]);
    SB();
    mfma_av<false>(ac2[0][4], wd[8][4], E[0][8]);
    SB();
    gload_vc<1024>(E[1][5], vo, eb[1] + 0);
    wait_vm<10>();
    mfma_av<false>(ac2[0][0], wd[9][0], E[0][9]);
    SB();
    v_max3(mx[0], E[0][9][0], E[0][9][1]);
    mfma_av<false>(ac2[0][1], wd[9][1], E[0][9]);
    SB();
    v_max3(mx[1], E[0][9][2], E[0][9][3]);
    mfma_av<false>(ac2[0][2], wd[9][2], E[0][9]);
    SB();
    mfma_av<false>(ac2[0][3], wd[9][3], E[0][9]);
    SB();
    mfma_av<false>(ac2[0][4], wd[9][4], E[0][9]);
    SB();
    gload_vc<2048>(E[1][6], vo, eb[1] + 0);
    wait_vm<10>();
    mfma_av<false>(ac2[0][0], wd[10][0], E[0][10]);
    SB();
    v_max3(mx[0], E[0][10][0], E[0][10][1]);
    mfma_av<false>(ac2[0][1], wd[10][1], E[0][10]);
    SB();
    v_max3(mx[1], E[0][10][2], E[0][10][3]);
    mfma_av<false>(ac2[0][2], wd[10][2], E[0][10]);
    SB();
    mfma_av<false>(ac2[0][3], wd[10][3], E[0][10]);
    SB();
    mfma_av<false>(ac2[0][4], wd[10][4], E[0][10]);
    SB();
    gload_vc<3072>(E[1][7], vo, eb[1] + 0);
    wait_vm<10>();
    mfma_av<false>(ac2[0][0], wd[11][0], E[0][11]);
    SB();
    v_max3(mx[0], E[0][11][0], E[0][11][1]);
    mfma_av<false>(ac2[0][1], wd[11][1], E[0][11]);
    SB();
    v_max3(mx[1], E[0][11][2], E[0][11][3]);
    mfma_av<false>(ac2[0][2], wd[11][2], E[0][11]);
    SB();
    mfma_av<false>(ac2[0][3], wd[11][3], E[0][11]);
    SB();
    mfma_av<false>(ac2[0][4], wd[11][4], E[0][11]);
    SB();
    gload_vc<-4096>(E[1][8], vo, eb[1] + 8192);
    STAMP(4);
    wait_vm<10>();
    mfma_av<true>(ac2[1][0], wd[0][0], E[1][0]);
    SB();
    v_max3(mx[0], E[1][0][0], E[1][0][1]);
    mfma_av<true>(ac2[1][1], wd[0][1], E[1][0]);
    SB();
    v_max3(mx[1], E[1][0][2], E[1][0][3]);
    mfma_av<true>(ac2[1][2], wd[0][2], E[1][0]);
    SB();
    mfma_av<true>(ac2[1][3], wd[0][3], E[1][0]);
    SB();
    mfma_av<true>(ac2[1][4], wd[0][4], E[1][0]);
    SB();
    gload_vc<-3072>(E[1][9], vo, eb[1] + 8192);
    wait_vm<10>();
    mfma_av<false>(ac2[1][0], wd[1][0], E[1][1]);
    SB();
    v_max3(mx[0], E[1][1][0], E[1][1][1]);
    mfma_av<false>(ac2[1][1], wd[1][1], E[1][1]);
    SB();
    v_max3(mx[1], E[1][1][2], E[1][1][3]);
    mfma_av<false>(ac2[1][2], wd[1][2], E[1][1]);
    SB();
    mfma_av<false>(ac2[1][3], wd[1][3], E[1][1]);
    SB();
    mfma_av<false>(ac2[1][4], wd[1][4], E[1][1]);
    SB();
    gload_vc<-2048>(E[1][10], vo, eb[1] + 8192);
    wait_vm<10>();
    mfma_av<false>(ac2[1][0], wd[2][0], E[1][2]);
    SB();
    v_max3(mx[0], E[1][2][0], E[1][2][1]);
    mfma_av<false>(ac2[1][1], wd[2][1], E[1][2]);
    SB();
    v_max3(mx[1], E[1][2][2], E[1][2][3]);
    mfma_av<false>(ac2[1][2], wd[2][2], E[1][2]);
    SB();
    mfma_av<false>(ac2[1][3], wd[2][3], E[1][2]);
    SB();
    lds_write4<0>(lr, ac2[0][0]);
    mfma_av<false>(ac2[1][4], wd[2][4], E[1][2]);
    SB();
    gload_vc<-1024>(E[1][11], vo, eb[1] + 8192);
    wait_vm<10>();
    mfma_av<false>(ac2[1][0], wd[3][0], E[1][3]);
    SB();
    v_max3(mx[0], E[1][3][0], E[1][3][1]);
    mfma_av<false>(ac2[1][1], wd[3][1], E[1][3]);
    SB();
    v_max3(mx[1], E[1][3][2], E[1][3][3]);
    mfma_av<false>(ac2[1][2], wd[3][2], E[1][3]);
    SB();
    mfma_av<false>(ac2[1][3], wd[3][3], E[1][3]);
    SB();
    lds_write4<4096>(lr, ac2[0][1]);
    mfma_av<false>(ac2[1][4], wd[3][4], E[1][3]);
    SB();
    gload_vc<-4096>(E[2][0], vo, eb[2] + 0);
    wait_vm<8>();
    mfma_av<false>(ac2[1][0], wd[4][0], E[1][4]);
    SB();
    v_max3(mx[0], E[1][4][0], E[1][4][1]);
    mfma_av<false>(ac2[1][1], wd[4][1], E[1][4]);
    SB();
    v_max3(mx[1], E[1][4][2], E[1][4][3]);
    mfma_av<false>(ac2[1][2], wd[4][2], E[1][4]);
    SB();
    mfma_av<false>(ac2[1][3], wd[4][3], E[1][4]);
    SB();
    lds_write4<8192>(lr, ac2[0][2]);
    mfma_av<false>(ac2[1][4], wd[4][4], E[1][4]);
    SB();
    gload_vc<-3072>(E[2][1], vo, eb[2] + 0);
    wait_vm<8>();
    mfma_av<false>(ac2[1][0], wd[5][0], E[1][5]);
    SB();
    v_max3(mx[0], E[1][5][0], E[1][5][1]);
    mfma_av<false>(ac2[1][1], wd[5][1], E[1][5]);
    SB();
    v_max3(mx[1], E[1][5][2], E[1][5][3]);
    mfma_av<false>(ac2[1][2], wd[5][2], E[1][5]);
    SB();
    mfma_av<false>(ac2[1][3], wd[5][3], E[1][5]);
    SB();
    lds_write4<12288>(lr, ac2[0][3]);
    mfma_av<false>(ac2[1][4], wd[5][4], E[1][5]);
    SB();
    gload_vc<-2048>(E[2][2], vo, eb[2] + 0);
    wait_vm<8>();
    mfma_av<false>(ac2[1][0], wd[6][0], E[1][6]);
    SB();
    v_max3(mx[0], E[1][6][0], E[1][6][1]);
    mfma_av<false>(ac2[1][1], wd[6][1], E[1][6]);
    SB();
    v_max3(mx[1], E[1][6][2], E[1][6][3]);
    mfma_av<false>(ac2[1][2], wd[6][2], E[1][6]);
    SB();
    mfma_av<false>(ac2[1][3], wd[6][3], E[1][6]);
    SB();
    lds_write4<16384>(lr, ac2[0][4]);
    mfma_av<false>(ac2[1][4], wd[6][4], E[1][6]);
    SB();
    gload_vc<-1024>(E[2][3], vo, eb[2] + 0);
    wait_vm<8>();
    mfma_av<false>(ac2[1][0], wd[7][0], E[1][7]);
    SB();
    v_max3(mx[0], E[1][7][0], E[1][7][1]);
    mfma_av<false>(ac2[1][1], wd[7][1], E[1][7]);
    SB();
    v_max3(mx[1], E[1][7][2], E[1][7][3]);
    mfma_av<false>(ac2[1][2], wd[7][2], E[1][7]);
    SB();
    mfma_av<false>(ac2[1][3], wd[7][3], E[1][7]);
    SB();
    mfma_av<false>(ac2[1][4], wd[7][4], E[1][7]);
    SB();
    gload_vc<0>(E[2][4], vo, eb[2] + 0);
    wait_vm<8>();
    mfma_av<false>(ac2[1][0], wd[8][0], E[1][8]);
    SB();
    v_max3(mx[0], E[1][8][0], E[1][8][1]);
    mfma_av<false>(ac2[1][1], wd[8][1], E[1][8]);
    SB();
    v_max3(mx[1], E[1][8][2], E[1][8][3]);
    mfma_av<false>(ac2[1][2], wd[8][2], E[1][8]);
    SB();
    mfma_av<false>(ac2[1][3], wd[8][3], E[1][8]);
    SB();
    mfma_av<false>(ac2[1][4], wd[8][4], E[1][8]);
    SB();
    gload_vc<1024>(E[2][5], vo, eb[2] + 0);
    wait_vm<8>();
    mfma_av<false>(ac2[1][0], wd[9][0], E[1][9]);
    SB();
    v_max3(mx[0], E[1][9][0], E[1][9][1]);
    mfma_av<false>(ac2[1][1], wd[9][1], E[1][9]);
    SB();
    v_max3(mx[1], E[1][9][2], E[1][9][3]);
    mfma_av<false>(ac2[1][2], wd[9][2], E[1][9]);
    SB();
    mfma_av<false>(ac2[1][3], wd[9][3], E[1][9]);
    SB();
    mfma_av<false>(ac2[1][4], wd[9][4], E[1][9]);
    SB();
    gload_vc<2048>(E[2][6], vo, eb[2] + 0);
    wait_vm<8>();
    mfma_av<false>(ac2[1][0], wd[10][0], E[1][10]);
    SB();
    v_max3(mx[0], E[1][10][0], E[1][10][1]);
    mfma_av<false>(ac2[1][1], wd[10][1], E[1][10]);
    SB();
    v_max3(mx[1], E[1][10][2], E[1][10][3]);
    mfma_av<false>(ac2[1][2], wd[10][2], E[1][10]);
    SB();
    mfma_av<false>(ac2[1][3], wd[10][3], E[1][10]);
    SB();
    mfma_av<false>(ac2[1][4], wd[10][4], E[1][10]);
    SB();
    gload_vc<3072>(E[2][7], vo, eb[2] + 0);
    wait_vm<8>();
    mfma_av<false>(ac2[1][0], wd[11][0], E[1][11]);
    SB();
    v_max3(mx[0], E[1][11][0], E[1][11][1]);
    mfma_av<false>(ac2[1][1], wd[11][1], E[1][11]);
    SB();
    v_max3(mx[1], E[1][11][2], E[1][11][3]);
    mfma_av<false>(ac2[1][2], wd[11][2], E[1][11]);
    SB();
    mfma_av<false>(ac2[1][3], wd[11][3], E[1][11]);
    SB();
    mfma_av<false>(ac2[1][4], wd[11][4], E[1][11]);
    SB();
    gload_vc<-4096>(E[2][8], vo, eb[2] + 8192);
    wait_vm<8>();
    mfma_av<true>(ac2[2][0], wd[0][0], E[2][0]);
    SB();
    v_max3(mx[0], E[2][0][0], E[2][0][1]);
    mfma_av<true>(ac2[2][1], wd[0][1], E[2][0]);
    SB();
    v_max3(mx[1], E[2][0][2], E[2][0][3]);
    mfma_av<true>(ac2[2][2], wd[0][2], E[2][0]);
    SB();
    mfma_av<true>(ac2[2][3], wd[0][3], E[2][0]);
    SB();
    mfma_av<true>(ac2[2][4], wd[0][4], E[2][0]);
    SB();
    gload_vc<-3072>(E[2][9], vo, eb[2] + 8192);
    wait_vm<8>();
    mfma_av<false>(ac2[2][0], wd[1][0], E[2][1]);
    SB();
    v_max3(mx[0], E[2][1][0], E[2][1][1]);
    mfma_av<false>(ac2[2][1], wd[1][1], E[2][1]);
    SB();
    v_max3(mx[1], E[2][1][2], E[2][1][3]);
    mfma_av<false>(ac2[2][2], wd[1][2], E[2][1]);
    SB();
    mfma_av<false>(ac2[2][3], wd[1][3], E[2][1]);
    SB();
    mfma_av<false>(ac2[2][4], wd[1][4], E[2][1]);
    SB();
    gload_vc<-2048>(E[2][10], vo, eb[2] + 8192);
    wait_vm<8>();
    mfma_av<false>(ac2[2][0], wd[2][0], E[2][2]);
    SB();
    v_max3(mx[0], E[2][2][0], E[2][2][1]);
    mfma_av<false>(ac2[2][1], wd[2][1], E[2][2]);
    SB();
    v_max3(mx[1], E[2][2][2], E[2][2][3]);
    mfma_av<false>(ac2[2][2], wd[2][2], E[2][2]);
    SB();
    mfma_av<false>(ac2[2][3], wd[2][3], E[2][2]);
    SB();
    lds_write4<20480>(lr, ac2[1][0]);
    mfma_av<false>(ac2[2][4], wd[2][4], E[2][2]);
    SB();
    gload_vc<-1024>(E[2][11], vo, eb[2] + 8192);
    wait_vm<8>();
    mfma_av<false>(ac2[2][0], wd[3][0], E[2][3]);
    SB();
    v_max3(mx[0], E[2][3][0], E[2][3][1]);
    mfma_av<false>(ac2[2][1], wd[3][1], E[2][3]);
    SB();
    v_max3(mx[1], E[2][3][2], E[2][3][3]);
    mfma_av<false>(ac2[2][2], wd[3][2], E[2][3]);
    SB();
    mfma_av<false>(ac2[2][3], wd[3][3], E[2][3]);
    SB();
    lds_write4<24576>(lr, ac2[1][1]);
    mfma_av<false>(ac2[2][4], wd[3][4], E[2][3]);
    SB();
    wait_vm<7>();
    mfma_av<false>(ac2[2][0], wd[4][0], E[2][4]);
    SB();
    v_max3(mx[0], E[2][4][0], E[2][4][1]);
    mfma_av<false>(ac2[2][1], wd[4][1], E[2][4]);
    SB();
    v_max3(mx[1], E[2][4][2], E[2][4][3]);
    mfma_av<false>(ac2[2][2], wd[4][2], E[2][4]);
    SB();
    mfma_av<false>(ac2[2][3], wd[4][3], E[2][4]);
    SB();
    lds_write4<28672>(lr, ac2[1][2]);
    mfma_av<false>(ac2[2][4], wd[4][4], E[2][4]);
    SB();
    wait_vm<6>();
    mfma_av<false>(ac2[2][0], wd[5][0], E[2][5]);
    SB();
    v_max3(mx[0], E[2][5][0], E[2][5][1]);
    mfma_av<false>(ac2[2][1], wd[5][1], E[2][5]);
    SB();
    v_max3(mx[1], E[2][5][2], E[2][5][3]);
    mfma_av<false>(ac2[2][2], wd[5][2], E[2][5]);
    SB();
    mfma_av<false>(ac2[2][3], wd[5][3], E[2][5]);
    SB();
    lds_write4<32768>(lr, ac2[1][3]);
    mfma_av<false>(ac2[2][4], wd[5][4], E[2][5]);
    SB();
    gload_vc<-4096>(E[3][0], vo, eb[3] + 0);
    gload_vc<-3072>(E[3][1], vo, eb[3] + 0);
    wait_vm<7>();
    mfma_av<false>(ac2[2][0], wd[6][0], E[2][6]);
    SB();
    v_max3(mx[0], E[2][6][0], E[2][6][1]);
    mfma_av<false>(ac2[2][1], wd[6][1], E[2][6]);
    SB();
    v_max3(mx[1], E[2][6][2], E[2][6][3]);
    mfma_av<false>(ac2[2][2], wd[6][2], E[2][6]);
    SB();
    mfma_av<false>(ac2[2][3], wd[6][3], E[2][6]);
    SB();
    lds_write4<36864>(lr, ac2[1][4]);
    mfma_av<false>(ac2[2][4], wd[6][4], E[2][6]);
    SB();
    gload_vc<-2048>(E[3][2], vo, eb[3] + 0);
    gload_vc<-1024>(E[3][3], vo, eb[3] + 0);
    wait_vm<8>();
    mfma_av<false>(ac2[2][0], wd[7][0], E[2][7]);
    SB();
    v_max3(mx[0], E[2][7][0], E[2][7][1]);
    mfma_av<false>(ac2[2][1], wd[7][1], E[2][7]);
    SB();
    v_max3(mx[1], E[2][7][2], E[2][7][3]);
    mfma_av<false>(ac2[2][2], wd[7][2], E[2][7]);
    SB();
    mfma_av<false>(ac2[2][3], wd[7][3], E[2][7]);
    SB();
    mfma_av<false>(ac2[2][4], wd[7][4], E[2][7]);
    SB();
    gload_vc<0>(E[3][4], vo, eb[3] + 0);
    gload_vc<1024>(E[3][5], vo, eb[3] + 0);
    wait_vm<9>();
    mfma_av<false>(ac2[2][0], wd[8][0], E[2][8]);
    SB();
    v_max3(mx[0], E[2][8][0], E[2][8][1]);
    mfma_av<false>(ac2[2][1], wd[8][1], E[2][8]);
    SB();
    v_max3(mx[1], E[2][8][2], E[2][8][3]);
    mfma_av<false>(ac2[2][2], wd[8][2], E[2][8]);
    SB();
    mfma_av<false>(ac2[2][3], wd[8][3], E[2][8]);
    SB();
    mfma_av<false>(ac2[2][4], wd[8][4], E[2][8]);
    SB();
    gload_vc<2048>(E[3][6], vo, eb[3] + 0);
    gload_vc<3072>(E[3][7], vo, eb[3] + 0);
    wait_vm<10>();
    mfma_av<false>(ac2[2][0], wd[9][0], E[2][9]);
    SB();
    v_max3(mx[0], E[2][9][0], E[2][9][1]);
    mfma_av<false>(ac2[2][1], wd[9][1], E[2][9]);
    SB();
    v_max3(mx[1], E[2][9][2], E[2][9][3]);
    mfma_av<false>(ac2[2][2], wd[9][2], E[2][9]);
    SB();
    mfma_av<false>(ac2[2][3], wd[9][3], E[2][9]);
    SB();
    mfma_av<false>(ac2[2][4], wd[9][4], E[2][9]);
    SB();
    gload_vc<-4096>(E[3][8], vo, eb[3] + 8192);
    gload_vc<-3072>(E[3][9], vo, eb[3] + 8192);
    wait_vm<11>();
    mfma_av<false>(ac2[2][0], wd[10][0], E[2][10]);
    SB();
    v_max3(mx[0], E[2][10][0], E[2][10][1]);
    mfma_av<false>(ac2[2][1], wd[10][1], E[2][10]);
    SB();
    v_max3(mx[1], E[2][10][2], E[2][10][3]);
    mfma_av<false>(ac2[2][2], wd[10][2], E[2][10]);
    SB();
    mfma_av<false>(ac2[2][3], wd[10][3], E[2][10]);
    SB();
    mfma_av<false>(ac2[2][4], wd[10][4], E[2][10]);
    SB();
    gload_vc<-2048>(E[3][10], vo, eb[3] + 8192);
    gload_vc<-1024>(E[3][11], vo, eb[3] + 8192);
    wait_vm<12>();
    mfma_av<false>(ac2[2][0], wd[11][0], E[2][11]);
    SB();
    v_max3(mx[0], E[2][11][0], E[2][11][1]);
    mfma_av<false>(ac2[2][1], wd[11][1], E[2][11]);
    SB();
    v_max3(mx[1], E[2][11][2], E[2][11][3]);
    mfma_av<false>(ac2[2][2], wd[11][2], E[2][11]);
    SB();
    mfma_av<false>(ac2[2][3], wd[11][3], E[2][11]);
    SB();
    mfma_av<false>(ac2[2][4], wd[11][4], E[2][11]);
    SB();
    asm volatile("s_nop 7\n\ts_nop 7" : "+v"(ac2[2][0]), "+v"(ac2[2][1]), "+v"(ac2[2][2]), "+v"(ac2[2][3]), "+v"(ac2[2][4])::"memory");
    lds_write4<40960>(lr, ac2[2][0]);
    lds_write4<45056>(lr, ac2[2][1]);
    lds_write4<49152>(lr, ac2[2][2]);
    lds_write4<53248>(lr, ac2[2][3]);
    lds_write4<57344>(lr, ac2[2][4]);
    lu_init();
    BAR();
    lds_readf<0>(R[0][0], lu[0]);
    lds_readf<1024>(R[0][1], lu[0]);
    lds_readf<2048>(R[0][2], lu[0]);
    lds_readf<3072>(R[0][3], lu[0]);
    lds_readf<0>(R[1][0], lu[1]);
    lds_readf<1024>(R[1][1], lu[1]);
    lds_readf<2048>(R[1][2], lu[1]);
    lds_readf<3072>(R[1][3], lu[1]);
    lds_readf<0>(R[2][0], lu[2]);
    lds_readf<1024>(R[2][1], lu[2]);
    lds_readf<2048>(R[2][2], lu[2]);
    lds_readf<3072>(R[2][3], lu[2]);
    lds_readf<0>(R[3][0], lu[3]);
    lds_readf<1024>(R[3][1], lu[3]);
    lds_readf<2048>(R[3][2], lu[3]);
    lds_readf<3072>(R[3][3], lu[3]);
    wait_lgkm<12>();
    tie(R[0][0]); tie(R[0][1]); tie(R[0][2]); tie(R[0][3]); tie(h[0]);
    v_add3(X[0][0], R[0][0][0], R[0][1][0]);
    v_add3(X[0][1], R[0][0][1], R[0][1][1]);
    v_add3(X[0][2], R[0][0][2], R[0][1][2]);
    v_add3(X[0][3], R[0][0][3], R[0][1][3]);
    v_add3(Y[0][0], R[0][2][0], R[0][3][0]);
    v_add3(Y[0][1], R[0][2][1], R[0][3][1]);
    v_add3(Y[0][2], R[0][2][2], R[0][3][2]);
    v_add3(Y[0][3], R[0][2][3], R[0][3][3]);
    v_lo(Z[0][0], h[0][0]);
    v_hi(Z[0][1], h[0][0]);
    v_lo(Z[0][2], h[0][1]);
    v_hi(Z[0][3], h[0][1]);
    v_add2(X[0][0], Y[0][0]);
    v_add2(X[0][1], Y[0][1]);
    v_add2(X[0][2], Y[0][2]);
    v_add2(X[0][3], Y[0][3]);
    v_pack(op[0][0], X[0][0], X[0][1]);
    v_pack(op[0][1], X[0][2], X[0][3]);
    v_lo(Y[0][0], op[0][0]);
    v_hi(Y[0][1], op[0][0]);
    v_lo(Y[0][2], op[0][1]);
    v_hi(Y[0][3], op[0][1]);
    v_add2(Z[0][0], Y[0][0]);
    v_add2(Z[0][1], Y[0][1]);
    v_add2(Z[0][2], Y[0][2]);
    v_add2(Z[0][3], Y[0][3]);
    v_pack(op[0][0], Z[0][0], Z[0][1]);
    v_pack(op[0][1], Z[0][2], Z[0][3]);
    gstore2<0>(ou[0], u2{op[0][0], op[0][1]}, out);
    wait_lgkm<8>();
    tie(R[1][0]); tie(R[1][1]); tie(R[1][2]); tie(R[1][3]); tie(h[1]);
    v_add3(X[1][0], R[1][0][0], R[1][1][0]);
    v_add3(X[1][1], R[1][0][1], R[1][1][1]);
    v_add3(X[1][2], R[1][0][2], R[1][1][2]);
    v_add3(X[1][3], R[1][0][3], R[1][1][3]);
    v_add3(Y[1][0], R[1][2][0], R[1][3][0]);
    v_add3(Y[1][1], R[1][2][1], R[1][3][1]);
    v_add3(Y[1][2], R[1][2][2], R[1][3][2]);
    v_add3(Y[1][3], R[1][2][3], R[1][3][3]);
    v_lo(Z[1][0], h[1][0]);
    v_hi(Z[1][1], h[1][0]);
    v_lo(Z[1][2], h[1][1]);
    v_hi(Z[1][3], h[1][1]);
    v_add2(X[1][0], Y[1][0]);
    v_add2(X[1][1], Y[1][1]);
    v_add2(X[1][2], Y[1][2]);
    v_add2(X[1][3], Y[1][3]);
    v_pack(op[1][0], X[1][0], X[1][1]);
    v_pack(op[1][1], X[1][2], X[1][3]);
    v_lo(Y[1][0], op[1][0]);
    v_hi(Y[1][1], op[1][0]);
    v_lo(Y[1][2], op[1][1]);
    v_hi(Y[1][3], op[1][1]);
    v_add2(Z[1][0], Y[1][0]);
    v_add2(Z[1][1], Y[1][1]);
    v_add2(Z[1][2], Y[1][2]);
    v_add2(Z[1][3], Y[1][3]);
    v_pack(op[1][0], Z[1][0], Z[1][1]);
    v_pack(op[1][1], Z[1][2], Z[1][3]);
    gstore2<0>(ou[1], u2{op[1][0], op[1][1]}, out);
    STAMP(5);
    wait_vm<13>();
    mfma_av<true>(ac2[3][0], wd[0][0], E[3][0]);
    SB();
    v_max3(mx[0], E[3][0][0], E[3][0][1]);
    wait_lgkm<0>();
    tie(R[2][0]); tie(R[2][1]); tie(R[2][2]); tie(R[2][3]); tie(h[2]);
    v_add3(X[2][0], R[2][0][0], R[2][1][0]);
    mfma_av<true>(ac2[3][1], wd[0][1], E[3][0]);
    SB();
    v_max3(mx[1], E[3][0][2], E[3][0][3]);
    v_add3(X[2][1], R[2][0][1], R[2][1][1]);
    mfma_av<true>(ac2[3][2], wd[0][2], E[3][0]);
    SB();
    v_add3(X[2][2], R[2][0][2], R[2][1][2]);
    v_add3(X[2][3], R[2][0][3], R[2][1][3]);
    mfma_av<true>(ac2[3][3], wd[0][3], E[3][0]);
    SB();
    v_add3(Y[2][0], R[2][2][0], R[2][3][0]);
    v_add3(Y[2][1], R[2][2][1], R[2][3][1]);
    mfma_av<true>(ac2[3][4], wd[0][4], E[3][0]);
    SB();
    v_add3(Y[2][2], R[2][2][2], R[2][3][2]);
    v_add3(Y[2][3], R[2][2][3], R[2][3][3]);
    wait_vm<12>();
    mfma_av<false>(ac2[3][0], wd[1][0], E[3][1]);
    SB();
    v_max3(mx[0], E[3][1][0], E[3][1][1]);
    v_lo(Z[2][0], h[2][0]);
    mfma_av<false>(ac2[3][1], wd[1][1], E[3][1]);
    SB();
    v_max3(mx[1], E[3][1][2], E[3][1][3]);
    v_hi(Z[2][1], h[2][0]);
    mfma_av<false>(ac2[3][2], wd[1][2], E[3][1]);
    SB();
    v_lo(Z[2][2], h[2][1]);
    v_hi(Z[2][3], h[2][1]);
    mfma_av<false>(ac2[3][3], wd[1][3], E[3][1]);
    SB();
    v_add2(X[2][0], Y[2][0]);
    v_add2(X[2][1], Y[2][1]);
    mfma_av<false>(ac2[3][4], wd[1][4], E[3][1]);
    SB();
    v_add2(X[2][2], Y[2][2]);
    v_add2(X[2][3], Y[2][3]);
    wait_vm<11>();
    mfma_av<false>(ac2[3][0], wd[2][0], E[3][2]);
    SB();
    v_max3(mx[0], E[3][2][0], E[3][2][1]);
    v_pack(op[2][0], X[2][0], X[2][1]);
    mfma_av<false>(ac2[3][1], wd[2][1], E[3][2]);
    SB();
    v_max3(mx[1], E[3][2][2], E[3][2][3]);
    v_pack(op[2][1], X[2][2], X[2][3]);
    mfma_av<false>(ac2[3][2], wd[2][2], E[3][2]);
    SB();
    v_lo(Y[2][0], op[2][0]);
    v_hi(Y[2][1], op[2][0]);
    mfma_av<false>(ac2[3][3], wd[2][3], E[3][2]);
    SB();
    v_lo(Y[2][2], op[2][1]);
    v_hi(Y[2][3], op[2][1]);
    mfma_av<false>(ac2[3][4], wd[2][4], E[3][2]);
    SB();
    v_add2(Z[2][0], Y[2][0]);
    v_add2(Z[2][1], Y[2][1]);
    wait_vm<10>();
    mfma_av<false>(ac2[3][0], wd[3][0], E[3][3]);
    SB();
    v_max3(mx[0], E[3][3][0], E[3][3][1]);
    v_add2(Z[2][2], Y[2][2]);
    mfma_av<false>(ac2[3][1], wd[3][1], E[3][3]);
    SB();
    v_max3(mx[1], E[3][3][2], E[3][3][3]);
    v_add2(Z[2][3], Y[2][3]);
    mfma_av<false>(ac2[3][2], wd[3][2], E[3][3]);
    SB();
    v_pack(op[2][0], Z[2][0], Z[2][1]);
    v_pack(op[2][1], Z[2][2], Z[2][3]);
    mfma_av<false>(ac2[3][3], wd[3][3], E[3][3]);
    SB();
    gstore2<0>(ou[2], u2{op[2][0], op[2][1]}, out);
    tie(R[3][0]); tie(R[3][1]); tie(R[3][2]); tie(R[3][3]); tie(h[3]);
    mfma_av<false>(ac2[3][4], wd[3][4], E[3][3]);
    SB();
    v_add3(X[3][0], R[3][0][0], R[3][1][0]);
    v_add3(X[3][1], R[3][0][1], R[3][1][1]);
    wait_vm<10>();
    mfma_av<false>(ac2[3][0], wd[4][0], E[3][4]);
    SB();
    v_max3(mx[0], E[3][4][0], E[3][4][1]);
    v_add3(X[3][2], R[3][0][2], R[3][1][2]);
    mfma_av<false>(ac2[3][1], wd[4][1], E[3][4]);
    SB();
    v_max3(mx[1], E[3][4][2], E[3][4][3]);
    v_add3(X[3][3], R[3][0][3], R[3][1][3]);
    mfma_av<false>(ac2[3][2], wd[4][2], E[3][4]);
    SB();
    v_add3(Y[3][0], R[3][2][0], R[3][3][0]);
    v_add3(Y[3][1], R[3][2][1], R[3][3][1]);
    mfma_av<false>(ac2[3][3], wd[4][3], E[3][4]);
    SB();
    v_add3(Y[3][2], R[3][2][2], R[3][3][2]);
    v_add3(Y[3][3], R[3][2][3], R[3][3][3]);
    mfma_av<false>(ac2[3][4], wd[4][4], E[3][4]);
    SB();
    v_lo(Z[3][0], h[3][0]);
    v_hi(Z[3][1], h[3][0]);
    wait_vm<9>();
    mfma_av<false>(ac2[3][0], wd[5][0], E[3][5]);
    SB();
    v_max3(mx[0], E[3][5][0], E[3][5][1]);
    v_lo(Z[3][2], h[3][1]);
    mfma_av<false>(ac2[3][1], wd[5][1], E[3][5]);
    SB();
    v_max3(mx[1], E[3][5][2], E[3][5][3]);
    v_hi(Z[3][3], h[3][1]);
    mfma_av<false>(ac2[3][2], wd[5][2], E[3][5]);
    SB();
    v_add2(X[3][0], Y[3][0]);
    v_add2(X[3][1], Y[3][1]);
    mfma_av<false>(ac2[3][3], wd[5][3], E[3][5]);
    SB();
    v_add2(X[3][2], Y[3][2]);
    v_add2(X[3][3], Y[3][3]);
    mfma_av<false>(ac2[3][4], wd[5][4], E[3][5]);
    SB();
    v_pack(op[3][0], X[3][0], X[3][1]);
    v_pack(op[3][1], X[3][2], X[3][3]);
    wait_vm<8>();
    mfma_av<false>(ac2[3][0], wd[6][0], E[3][6]);
    SB();
    v_max3(mx[0], E[3][6][0], E[3][6][1]);
    v_lo(Y[3][0], op[3][0]);
    mfma_av<false>(ac2[3][1], wd[6][1], E[3][6]);
    SB();
    v_max3(mx[1], E[3][6][2], E[3][6][3]);
    v_hi(Y[3][1], op[3][0]);
    mfma_av<false>(ac2[3][2], wd[6][2], E[3][6]);
    SB();
    v_lo(Y[3][2], op[3][1]);
    v_hi(Y[3][3], op[3][1]);
    mfma_av<false>(ac2[3][3], wd[6][3], E[3][6]);
    SB();
    v_add2(Z[3][0], Y[3][0]);
    v_add2(Z[3][1], Y[3][1]);
    mfma_av<false>(ac2[3][4], wd[6][4], E[3][6]);
    SB();
    v_add2(Z[3][2], Y[3][2]);
    v_add2(Z[3][3], Y[3][3]);
    wait_vm<7>();
    mfma_av<false>(ac2[3][0], wd[7][0], E[3][7]);
    SB();
    v_max3(mx[0], E[3][7][0], E[3][7][1]);
    v_pack(op[3][0], Z[3][0], Z[3][1]);
    mfma_av<false>(ac2[3][1], wd[7][1], E[3][7]);
    SB();
    v_max3(mx[1], E[3][7][2], E[3][7][3]);
    v_pack(op[3][1], Z[3][2], Z[3][3]);
    mfma_av<false>(ac2[3][2], wd[7][2], E[3][7]);
    SB();
    gstore2<0>(ou[3], u2{op[3][0], op[3][1]}, out);
    mfma_av<false>(ac2[3][3], wd[7][3], E[3][7]);
    SB();
    mfma_av<false>(ac2[3][4], wd[7][4], E[3][7]);
    SB();
    wait_vm<7>();
    mfma_av<false>(ac2[3][0], wd[8][0], E[3][8]);
    SB();
    v_max3(mx[0], E[3][8][0], E[3][8][1]);
    mfma_av<false>(ac2[3][1], wd[8][1], E[3][8]);
    SB();
    v_max3(mx[1], E[3][8][2], E[3][8][3]);
    mfma_av<false>(ac2[3][2], wd[8][2], E[3][8]);
    SB();
    mfma_av<false>(ac2[3][3], wd[8][3], E[3][8]);
    SB();
    mfma_av<false>(ac2[3][4], wd[8][4], E[3][8]);
    SB();
    wait_vm<6>();
    mfma_av<false>(ac2[3][0], wd[9][0], E[3][9]);
    SB();
    v_max3(mx[0], E[3][9][0], E[3][9][1]);
    mfma_av<false>(ac2[3][1], wd[9][1], E[3][9]);
    SB();
    v_max3(mx[1], E[3][9][2], E[3][9][3]);
    mfma_av<false>(ac2[3][2], wd[9][2], E[3][9]);
    SB();
    mfma_av<false>(ac2[3][3], wd[9][3], E[3][9]);
    SB();
    mfma_av<false>(ac2[3][4], wd[9][4], E[3][9]);
    SB();
    wait_vm<5>();
    mfma_av<false>(ac2[3][0], wd[10][0], E[3][10]);
    SB();
    v_max3(mx[0], E[3][10][0], E[3][10][1]);
    mfma_av<false>(ac2[3][1], wd[10][1], E[3][10]);
    SB();
    v_max3(mx[1], E[3][10][2], E[3][10][3]);
    mfma_av<false>(ac2[3][2], wd[10][2], E[3][10]);
    SB();
    mfma_av<false>(ac2[3][3], wd[10][3], E[3][10]);
    SB();
    mfma_av<false>(ac2[3][4], wd[10][4], E[3][10]);
    SB();
    wait_vm<4>();
    mfma_av<false>(ac2[3][0], wd[11][0], E[3][11]);
    SB();
    v_max3(mx[0], E[3][11][0], E[3][11][1]);
    mfma_av<false>(ac2[3][1], wd[11][1], E[3][11]);
    SB();
    v_max3(mx[1], E[3][11][2], E[3][11][3]);
    mfma_av<false>(ac2[3][2], wd[11][2], E[3][11]);
    SB();
    mfma_av<false>(ac2[3][3], wd[11][3], E[3][11]);
    SB();
    mfma_av<false>(ac2[3][4], wd[11][4], E[3][11]);
    SB();
  bool bad = __builtin_amdgcn_ballot_w64(max(mx[0], mx[1]) == 0xffffffffu) != 0;
  asm volatile("s_nop 7" : "+v"(ac2[3][0]), "+v"(ac2[3][1]), "+v"(ac2[3][2]), "+v"(ac2[3][3]), "+v"(ac2[3][4])::"memory");
  static_for<0, 5>([&](auto I) { lds_write4<I * 4096>(lr + 61440, ac2[3][I]); });
  lds_write<0>(lbase + 81920 + w * 4, bad ? 1u : 0u);
  BAR();
  u4 fb;
  f4 L[2][4];
  lds_read<0>(fb, lbase + 81920);
  static_for<0, 8>([&](auto I) { lds_readf<(I & 3) * 1024>(L[I >> 2][I & 3], lu[4 + (I >> 2)]); });
  wait_lgkm<0>();
  tie(fb);
  static_for<0, 8>([&](auto I) { tie(L[I >> 2][I & 3]); });
  static_for<0, 6>([&](auto N) { tie(h[N]); });
  if (__builtin_expect(__builtin_amdgcn_readfirstlane(fb[0] | fb[1] | fb[2] | fb[3]) != 0, 0)) {
    for (int tries = 0; bad; ++tries) {
      fl[0] = fl[1] = 0xffffffffu;
      gate(fl[0], fo, gbase, 0xffffffffffffull);
      gate(fl[1], fo, gbase + 196608, 0xffffffffffffull);
      if (__builtin_amdgcn_ballot_w64(max(fl[0], fl[1]) == 0xffffffffu) != 0 || tries >= 8) act_redo(AF, W1F, ACT, mt0, w * 4, lane);
      mx[0] = mx[1] = 0;
      u4 E[4][12];
    gload_vc<-4096>(E[0][0], vo, eb[0] + 0);
    gload_vc<-3072>(E[0][1], vo, eb[0] + 0);
    gload_vc<-2048>(E[0][2], vo, eb[0] + 0);
    gload_vc<-1024>(E[0][3], vo, eb[0] + 0);
    gload_vc<0>(E[0][4], vo, eb[0] + 0);
    gload_vc<1024>(E[0][5], vo, eb[0] + 0);
    gload_vc<2048>(E[0][6], vo, eb[0] + 0);
    gload_vc<3072>(E[0][7], vo, eb[0] + 0);
    gload_vc<-4096>(E[0][8], vo, eb[0] + 8192);
    gload_vc<-3072>(E[0][9], vo, eb[0] + 8192);
    gload_vc<-2048>(E[0][10], vo, eb[0] + 8192);
    gload_vc<-1024>(E[0][11], vo, eb[0] + 8192);
    wait_vm<11>();
    mfma_av<true>(ac2[0][0], wd[0][0], E[0][0]);
    v_max3(mx[0], E[0][0][0], E[0][0][1]);
    mfma_av<true>(ac2[0][1], wd[0][1], E[0][0]);
    v_max3(mx[1], E[0][0][2], E[0][0][3]);
    mfma_av<true>(ac2[0][2], wd[0][2], E[0][0]);
    mfma_av<true>(ac2[0][3], wd[0][3], E[0][0]);
    mfma_av<true>(ac2[0][4], wd[0][4], E[0][0]);
    gload_vc<-4096>(E[1][0], vo, eb[1] + 0);
    wait_vm<11>();
    mfma_av<false>(ac2[0][0], wd[1][0], E[0][1]);
    v_max3(mx[0], E[0][1][0], E[0][1][1]);
    mfma_av<false>(ac2[0][1], wd[1][1], E[0][1]);
    v_max3(mx[1], E[0][1][2], E[0][1][3]);
    mfma_av<false>(ac2[0][2], wd[1][2], E[0][1]);
    mfma_av<false>(ac2[0][3], wd[1][3], E[0][1]);
    mfma_av<false>(ac2[0][4], wd[1][4], E[0][1]);
    gload_vc<-3072>(E[1][1], vo, eb[1] + 0);
    wait_vm<11>();
    mfma_av<false>(ac2[0][0], wd[2][0], E[0][2]);
    v_max3(mx[0], E[0][2][0], E[0][2][1]);
    mfma_av<false>(ac2[0][1], wd[2][1], E[0][2]);
    v_max3(mx[1], E[0][2][2], E[0][2][3]);
    mfma_av<false>(ac2[0][2], wd[2][2], E[0][2]);
    mfma_av<false>(ac2[0][3], wd[2][3], E[0][2]);
    mfma_av<false>(ac2[0][4], wd[2][4], E[0][2]);
    gload_vc<-2048>(E[1][2], vo, eb[1] + 0);
    wait_vm<11>();
    mfma_av<false>(ac2[0][0], wd[3][0], E[0][3]);
    v_max3(mx[0], E[0][3][0], E[0][3][1]);
    mfma_av<false>(ac2[0][1], wd[3][1], E[0][3]);
    v_max3(mx[1], E[0][3][2], E[0][3][3]);
    mfma_av<false>(ac2[0][2], wd[3][2], E[0][3]);
    mfma_av<false>(ac2[0][3], wd[3][3], E[0][3]);
    mfma_av<false>(ac2[0][4], wd[3][4], E[0][3]);
    gload_vc<-1024>(E[1][3], vo, eb[1] + 0);
    wait_vm<11>();
    mfma_av<false>(ac2[0][0], wd[4][0], E[0][4]);
    v_max3(mx[0], E[0][4][0], E[0][4][1]);
    mfma_av<false>(ac2[0][1], wd[4][1], E[0][4]);
    v_max3(mx[1], E[0][4][2], E[0][4][3]);
    mfma_av<false>(ac2[0][2], wd[4][2], E[0][4]);
    mfma_av<false>(ac2[0][3], wd[4][3], E[0][4]);
    mfma_av<false>(ac2[0][4], wd[4][4], E[0][4]);
    gload_vc<0>(E[1][4], vo, eb[1] + 0);
    wait_vm<11>();
    mfma_av<false>(ac2[0][0], wd[5][0], E[0][5]);
    v_max3(mx[0], E[0][5][0], E[0][5][1]);
    mfma_av<false>(ac2[0][1], wd[5][1], E[0][5]);
    v_max3(mx[1], E[0][5][2], E[0][5][3]);
    mfma_av<false>(ac2[0][2], wd[5][2], E[0][5]);
    mfma_av<false>(ac2[0][3], wd[5][3], E[0][5]);
    mfma_av<false>(ac2[0][4], wd[5][4], E[0][5]);
    gload_vc<1024>(E[1][5], vo, eb[1] + 0);
    wait_vm<11>();
    mfma_av<false>(ac2[0][0], wd[6][0], E[0][6]);
    v_max3(mx[0], E[0][6][0], E[0][6][1]);
    mfma_av<false>(ac2[0][1], wd[6][1], E[0][6]);
    v_max3(mx[1], E[0][6][2], E[0][6][3]);
    mfma_av<false>(ac2[0][2], wd[6][2], E[0][6]);
    mfma_av<false>(ac2[0][3], wd[6][3], E[0][6]);
    mfma_av<false>(ac2[0][4], wd[6][4], E[0][6]);
    gload_vc<2048>(E[1][6], vo, eb[1] + 0);
    wait_vm<11>();
    mfma_av<false>(ac2[0][0], wd[7][0], E[0][7]);
    v_max3(mx[0], E[0][7][0], E[0][7][1]);
    mfma_av<false>(ac2[0][1], wd[7][1], E[0][7]);
    v_max3(mx[1], E[0][7][2], E[0][7][3]);
    mfma_av<false>(ac2[0][2], wd[7][2], E[0][7]);
    mfma_av<false>(ac2[0][3], wd[7][3], E[0][7]);
    mfma_av<false>(ac2[0][4], wd[7][4], E[0][7]);
    gload_vc<3072>(E[1][7], vo, eb[1] + 0);
    wait_vm<11>();
    mfma_av<false>(ac2[0][0], wd[8][0], E[0][8]);
    v_max3(mx[0], E[0][8][0], E[0][8][1]);
    mfma_av<false>(ac2[0][1], wd[8][1], E[0][8]);
    v_max3(mx[1], E[0][8][2], E[0][8][3]);
    mfma_av<false>(ac2[0][2], wd[8][2], E[0][8]);
    mfma_av<false>(ac2[0][3], wd[8][3], E[0][8]);
    mfma_av<false>(ac2[0][4], wd[8][4], E[0][8]);
    gload_vc<-4096>(E[1][8], vo, eb[1] + 8192);
    wait_vm<11>();
    mfma_av<false>(ac2[0][0], wd[9][0], E[0][9]);
    v_max3(mx[0], E[0][9][0], E[0][9][1]);
    mfma_av<false>(ac2[0][1], wd[9][1], E[0][9]);
    v_max3(mx[1], E[0][9][2], E[0][9][3]);
    mfma_av<false>(ac2[0][2], wd[9][2], E[0][9]);
    mfma_av<false>(ac2[0][3], wd[9][3], E[0][9]);
    mfma_av<false>(ac2[0][4], wd[9][4], E[0][9]);
    gload_vc<-3072>(E[1][9], vo, eb[1] + 8192);
    wait_vm<11>();
    mfma_av<false>(ac2[0][0], wd[10][0], E[0][10]);
    v_max3(mx[0], E[0][10][0], E[0][10][1]);
    mfma_av<false>(ac2[0][1], wd[10][1], E[0][10]);
    v_max3(mx[1], E[0][10][2], E[0][10][3]);
    mfma_av<false>(ac2[0][2], wd[10][2], E[0][10]);
    mfma_av<false>(ac2[0][3], wd[10][3], E[0][10]);
    mfma_av<false>(ac2[0][4], wd[10][4], E[0][10]);
    gload_vc<-2048>(E[1][10], vo, eb[1] + 8192);
    wait_vm<11>();
    mfma_av<false>(ac2[0][0], wd[11][0], E[0][11]);
    v_max3(mx[0], E[0][11][0], E[0][11][1]);
    mfma_av<false>(ac2[0][1], wd[11][1], E[0][11]);
    v_max3(mx[1], E[0][11][2], E[0][11][3]);
    mfma_av<false>(ac2[0][2], wd[11][2], E[0][11]);
    mfma_av<false>(ac2[0][3], wd[11][3], E[0][11]);
    mfma_av<false>(ac2[0][4], wd[11][4], E[0][11]);
    gload_vc<-1024>(E[1][11], vo, eb[1] + 8192);
    wait_vm<11>();
    mfma_av<true>(ac2[1][0], wd[0][0], E[1][0]);
    v_max3(mx[0], E[1][0][0], E[1][0][1]);
    mfma_av<true>(ac2[1][1], wd[0][1], E[1][0]);
    v_max3(mx[1], E[1][0][2], E[1][0][3]);
    mfma_av<true>(ac2[1][2], wd[0][2], E[1][0]);
    mfma_av<true>(ac2[1][3], wd[0][3], E[1][0]);
    mfma_av<true>(ac2[1][4], wd[0][4], E[1][0]);
    gload_vc<-4096>(E[2][0], vo, eb[2] + 0);
    wait_vm<11>();
    mfma_av<false>(ac2[1][0], wd[1][0], E[1][1]);
    v_max3(mx[0], E[1][1][0], E[1][1][1]);
    mfma_av<false>(ac2[1][1], wd[1][1], E[1][1]);
    v_max3(mx[1], E[1][1][2], E[1][1][3]);
    mfma_av<false>(ac2[1][2], wd[1][2], E[1][1]);
    mfma_av<false>(ac2[1][3], wd[1][3], E[1][1]);
    mfma_av<false>(ac2[1][4], wd[1][4], E[1][1]);
    gload_vc<-3072>(E[2][1], vo, eb[2] + 0);
    wait_vm<11>();
    mfma_av<false>(ac2[1][0], wd[2][0], E[1][2]);
    v_max3(mx[0], E[1][2][0], E[1][2][1]);
    mfma_av<false>(ac2[1][1], wd[2][1], E[1][2]);
    v_max3(mx[1], E[1][2][2], E[1][2][3]);
    mfma_av<false>(ac2[1][2], wd[2][2], E[1][2]);
    mfma_av<false>(ac2[1][3], wd[2][3], E[1][2]);
    lds_write4<0>(lr, ac2[0][0]);
    mfma_av<false>(ac2[1][4], wd[2][4], E[1][2]);
    gload_vc<-2048>(E[2][2], vo, eb[2] + 0);
    wait_vm<11>();
    mfma_av<false>(ac2[1][0], wd[3][0], E[1][3]);
    v_max3(mx[0], E[1][3][0], E[1][3][1]);
    mfma_av<false>(ac2[1][1], wd[3][1], E[1][3]);
    v_max3(mx[1], E[1][3][2], E[1][3][3]);
    mfma_av<false>(ac2[1][2], wd[3][2], E[1][3]);
    mfma_av<false>(ac2[1][3], wd[3][3], E[1][3]);
    lds_write4<4096>(lr, ac2[0][1]);
    mfma_av<false>(ac2[1][4], wd[3][4], E[1][3]);
    gload_vc<-1024>(E[2][3], vo, eb[2] + 0);
    wait_vm<11>();
    mfma_av<false>(ac2[1][0], wd[4][0], E[1][4]);
    v_max3(mx[0], E[1][4][0], E[1][4][1]);
    mfma_av<false>(ac2[1][1], wd[4][1], E[1][4]);
    v_max3(mx[1], E[1][4][2], E[1][4][3]);
    mfma_av<false>(ac2[1][2], wd[4][2], E[1][4]);
    mfma_av<false>(ac2[1][3], wd[4][3], E[1][4]);
    lds_write4<8192>(lr, ac2[0][2]);
    mfma_av<false>(ac2[1][4], wd[4][4], E[1][4]);
    gload_vc<0>(E[2][4], vo, eb[2] + 0);
    wait_vm<11>();
    mfma_av<false>(ac2[1][0], wd[5][0], E[1][5]);
    v_max3(mx[0], E[1][5][0], E[1][5][1]);
    mfma_av<false>(ac2[1][1], wd[5][1], E[1][5]);
    v_max3(mx[1], E[1][5][2], E[1][5][3]);
    mfma_av<false>(ac2[1][2], wd[5][2], E[1][5]);
    mfma_av<false>(ac2[1][3], wd[5][3], E[1][5]);
    lds_write4<12288>(lr, ac2[0][3]);
    mfma_av<false>(ac2[1][4], wd[5][4], E[1][5]);
    gload_vc<1024>(E[2][5], vo, eb[2] + 0);
    wait_vm<11>();
    mfma_av<false>(ac2[1][0], wd[6][0], E[1][6]);
    v_max3(mx[0], E[1][6][0], E[1][6][1]);
    mfma_av<false>(ac2[1][1], wd[6][1], E[1][6]);
    v_max3(mx[1], E[1][6][2], E[1][6][3]);
    mfma_av<false>(ac2[1][2], wd[6][2], E[1][6]);
    mfma_av<false>(ac2[1][3], wd[6][3], E[1][6]);
    lds_write4<16384>(lr, ac2[0][4]);
    mfma_av<false>(ac2[1][4], wd[6][4], E[1][6]);
    gload_vc<2048>(E[2][6], vo, eb[2] + 0);
    wait_vm<11>();
    mfma_av<false>(ac2[1][0], wd[7][0], E[1][7]);
    v_max3(mx[0], E[1][7][0], E[1][7][1]);
    mfma_av<false>(ac2[1][1], wd[7][1], E[1][7]);
    v_max3(mx[1], E[1][7][2], E[1][7][3]);
    mfma_av<false>(ac2[1][2], wd[7][2], E[1][7]);
    mfma_av<false>(ac2[1][3], wd[7][3], E[1][7]);
    mfma_av<false>(ac2[1][4], wd[7][4], E[1][7]);
    gload_vc<3072>(E[2][7], vo, eb[2] + 0);
    wait_vm<11>();
    mfma_av<false>(ac2[1][0], wd[8][0], E[1][8]);
    v_max3(mx[0], E[1][8][0], E[1][8][1]);
    mfma_av<false>(ac2[1][1], wd[8][1], E[1][8]);
    v_max3(mx[1], E[1][8][2], E[1][8][3]);
    mfma_av<false>(ac2[1][2], wd[8][2], E[1][8]);
    mfma_av<false>(ac2[1][3], wd[8][3], E[1][8]);
    mfma_av<false>(ac2[1][4], wd[8][4], E[1][8]);
    gload_vc<-4096>(E[2][8], vo, eb[2] + 8192);
    wait_vm<11>();
    mfma_av<false>(ac2[1][0], wd[9][0], E[1][9]);
    v_max3(mx[0], E[1][9][0], E[1][9][1]);
    mfma_av<false>(ac2[1][1], wd[9][1], E[1][9]);
    v_max3(mx[1], E[1][9][2], E[1][9][3]);
    mfma_av<false>(ac2[1][2], wd[9][2], E[1][9]);
    mfma_av<false>(ac2[1][3], wd[9][3], E[1][9]);
    mfma_av<false>(ac2[1][4], wd[9][4], E[1][9]);
    gload_vc<-3072>(E[2][9], vo, eb[2] + 8192);
    wait_vm<11>();
    mfma_av<false>(ac2[1][0], wd[10][0], E[1][10]);
    v_max3(mx[0], E[1][10][0], E[1][10][1]);
    mfma_av<false>(ac2[1][1], wd[10][1], E[1][10]);
    v_max3(mx[1], E[1][10][2], E[1][10][3]);
    mfma_av<false>(ac2[1][2], wd[10][2], E[1][10]);
    mfma_av<false>(ac2[1][3], wd[10][3], E[1][10]);
    mfma_av<false>(ac2[1][4], wd[10][4], E[1][10]);
    gload_vc<-2048>(E[2][10], vo, eb[2] + 8192);
    wait_vm<11>();
    mfma_av<false>(ac2[1][0], wd[11][0], E[1][11]);
    v_max3(mx[0], E[1][11][0], E[1][11][1]);
    mfma_av<false>(ac2[1][1], wd[11][1], E[1][11]);
    v_max3(mx[1], E[1][11][2], E[1][11][3]);
    mfma_av<false>(ac2[1][2], wd[11][2], E[1][11]);
    mfma_av<false>(ac2[1][3], wd[11][3], E[1][11]);
    mfma_av<false>(ac2[1][4], wd[11][4], E[1][11]);
    gload_vc<-1024>(E[2][11], vo, eb[2] + 8192);
    wait_vm<11>();
    mfma_av<true>(ac2[2][0], wd[0][0], E[2][0]);
    v_max3(mx[0], E[2][0][0], E[2][0][1]);
    mfma_av<true>(ac2[2][1], wd[0][1], E[2][0]);
    v_max3(mx[1], E[2][0][2], E[2][0][3]);
    mfma_av<true>(ac2[2][2], wd[0][2], E[2][0]);
    mfma_av<true>(ac2[2][3], wd[0][3], E[2][0]);
    mfma_av<true>(ac2[2][4], wd[0][4], E[2][0]);
    gload_vc<-4096>(E[3][0], vo, eb[3] + 0);
    wait_vm<11>();
    mfma_av<false>(ac2[2][0], wd[1][0], E[2][1]);
    v_max3(mx[0], E[2][1][0], E[2][1][1]);
    mfma_av<false>(ac2[2][1], wd[1][1], E[2][1]);
    v_max3(mx[1], E[2][1][2], E[2][1][3]);
    mfma_av<false>(ac2[2][2], wd[1][2], E[2][1]);
    mfma_av<false>(ac2[2][3], wd[1][3], E[2][1]);
    mfma_av<false>(ac2[2][4], wd[1][4], E[2][1]);
    gload_vc<-3072>(E[3][1], vo, eb[3] + 0);
    wait_vm<11>();
    mfma_av<false>(ac2[2][0], wd[2][0], E[2][2]);
    v_max3(mx[0], E[2][2][0], E[2][2][1]);
    mfma_av<false>(ac2[2][1], wd[2][1], E[2][2]);
    v_max3(mx[1], E[2][2][2], E[2][2][3]);
    mfma_av<false>(ac2[2][2], wd[2][2], E[2][2]);
    mfma_av<false>(ac2[2][3], wd[2][3], E[2][2]);
    lds_write4<20480>(lr, ac2[1][0]);
    mfma_av<false>(ac2[2][4], wd[2][4], E[2][2]);
    gload_vc<-2048>(E[3][2], vo, eb[3] + 0);
    wait_vm<11>();
    mfma_av<false>(ac2[2][0], wd[3][0], E[2][3]);
    v_max3(mx[0], E[2][3][0], E[2][3][1]);
    mfma_av<false>(ac2[2][1], wd[3][1], E[2][3]);
    v_max3(mx[1], E[2][3][2], E[2][3][3]);
    mfma_av<false>(ac2[2][2], wd[3][2], E[2][3]);
    mfma_av<false>(ac2[2][3], wd[3][3], E[2][3]);
    lds_write4<24576>(lr, ac2[1][1]);
    mfma_av<false>(ac2[2][4], wd[3][4], E[2][3]);
    gload_vc<-1024>(E[3][3], vo, eb[3] + 0);
    wait_vm<11>();
    mfma_av<false>(ac2[2][0], wd[4][0], E[2][4]);
    v_max3(mx[0], E[2][4][0], E[2][4][1]);
    mfma_av<false>(ac2[2][1], wd[4][1], E[2][4]);
    v_max3(mx[1], E[2][4][2], E[2][4][3]);
    mfma_av<false>(ac2[2][2], wd[4][2], E[2][4]);
    mfma_av<false>(ac2[2][3], wd[4][3], E[2][4]);
    lds_write4<28672>(lr, ac2[1][2]);
    mfma_av<false>(ac2[2][4], wd[4][4], E[2][4]);
    gload_vc<0>(E[3][4], vo, eb[3] + 0);
    wait_vm<11>();
    mfma_av<false>(ac2[2][0], wd[5][0], E[2][5]);
    v_max3(mx[0], E[2][5][0], E[2][5][1]);
    mfma_av<false>(ac2[2][1], wd[5][1], E[2][5]);
    v_max3(mx[1], E[2][5][2], E[2][5][3]);
    mfma_av<false>(ac2[2][2], wd[5][2], E[2][5]);
    mfma_av<false>(ac2[2][3], wd[5][3], E[2][5]);
    lds_write4<32768>(lr, ac2[1][3]);
    mfma_av<false>(ac2[2][4], wd[5][4], E[2][5]);
    gload_vc<1024>(E[3][5], vo, eb[3] + 0);
    wait_vm<11>();
    mfma_av<false>(ac2[2][0], wd[6][0], E[2][6]);
    v_max3(mx[0], E[2][6][0], E[2][6][1]);
    mfma_av<false>(ac2[2][1], wd[6][1], E[2][6]);
    v_max3(mx[1], E[2][6][2], E[2][6][3]);
    mfma_av<false>(ac2[2][2], wd[6][2], E[2][6]);
    mfma_av<false>(ac2[2][3], wd[6][3], E[2][6]);
    lds_write4<36864>(lr, ac2[1][4]);
    mfma_av<false>(ac2[2][4], wd[6][4], E[2][6]);
    gload_vc<2048>(E[3][6], vo, eb[3] + 0);
    wait_vm<11>();
    mfma_av<false>(ac2[2][0], wd[7][0], E[2][7]);
    v_max3(mx[0], E[2][7][0], E[2][7][1]);
    mfma_av<false>(ac2[2][1], wd[7][1], E[2][7]);
    v_max3(mx[1], E[2][7][2], E[2][7][3]);
    mfma_av<false>(ac2[2][2], wd[7][2], E[2][7]);
    mfma_av<false>(ac2[2][3], wd[7][3], E[2][7]);
    mfma_av<false>(ac2[2][4], wd[7][4], E[2][7]);
    gload_vc<3072>(E[3][7], vo, eb[3] + 0);
    wait_vm<11>();
    mfma_av<false>(ac2[2][0], wd[8][0], E[2][8]);
    v_max3(mx[0], E[2][8][0], E[2][8][1]);
    mfma_av<false>(ac2[2][1], wd[8][1], E[2][8]);
    v_max3(mx[1], E[2][8][2], E[2][8][3]);
    mfma_av<false>(ac2[2][2], wd[8][2], E[2][8]);
    mfma_av<false>(ac2[2][3], wd[8][3], E[2][8]);
    mfma_av<false>(ac2[2][4], wd[8][4], E[2][8]);
    gload_vc<-4096>(E[3][8], vo, eb[3] + 8192);
    wait_vm<11>();
    mfma_av<false>(ac2[2][0], wd[9][0], E[2][9]);
    v_max3(mx[0], E[2][9][0], E[2][9][1]);
    mfma_av<false>(ac2[2][1], wd[9][1], E[2][9]);
    v_max3(mx[1], E[2][9][2], E[2][9][3]);
    mfma_av<false>(ac2[2][2], wd[9][2], E[2][9]);
    mfma_av<false>(ac2[2][3], wd[9][3], E[2][9]);
    mfma_av<false>(ac2[2][4], wd[9][4], E[2][9]);
    gload_vc<-3072>(E[3][9], vo, eb[3] + 8192);
    wait_vm<11>();
    mfma_av<false>(ac2[2][0], wd[10][0], E[2][10]);
    v_max3(mx[0], E[2][10][0], E[2][10][1]);
    mfma_av<false>(ac2[2][1], wd[10][1], E[2][10]);
    v_max3(mx[1], E[2][10][2], E[2][10][3]);
    mfma_av<false>(ac2[2][2], wd[10][2], E[2][10]);
    mfma_av<false>(ac2[2][3], wd[10][3], E[2][10]);
    mfma_av<false>(ac2[2][4], wd[10][4], E[2][10]);
    gload_vc<-2048>(E[3][10], vo, eb[3] + 8192);
    wait_vm<11>();
    mfma_av<false>(ac2[2][0], wd[11][0], E[2][11]);
    v_max3(mx[0], E[2][11][0], E[2][11][1]);
    mfma_av<false>(ac2[2][1], wd[11][1], E[2][11]);
    v_max3(mx[1], E[2][11][2], E[2][11][3]);
    mfma_av<false>(ac2[2][2], wd[11][2], E[2][11]);
    mfma_av<false>(ac2[2][3], wd[11][3], E[2][11]);
    mfma_av<false>(ac2[2][4], wd[11][4], E[2][11]);
    gload_vc<-1024>(E[3][11], vo, eb[3] + 8192);
    wait_vm<11>();
    mfma_av<true>(ac2[3][0], wd[0][0], E[3][0]);
    v_max3(mx[0], E[3][0][0], E[3][0][1]);
    mfma_av<true>(ac2[3][1], wd[0][1], E[3][0]);
    v_max3(mx[1], E[3][0][2], E[3][0][3]);
    mfma_av<true>(ac2[3][2], wd[0][2], E[3][0]);
    mfma_av<true>(ac2[3][3], wd[0][3], E[3][0]);
    mfma_av<true>(ac2[3][4], wd[0][4], E[3][0]);
    wait_vm<10>();
    mfma_av<false>(ac2[3][0], wd[1][0], E[3][1]);
    v_max3(mx[0], E[3][1][0], E[3][1][1]);
    mfma_av<false>(ac2[3][1], wd[1][1], E[3][1]);
    v_max3(mx[1], E[3][1][2], E[3][1][3]);
    mfma_av<false>(ac2[3][2], wd[1][2], E[3][1]);
    mfma_av<false>(ac2[3][3], wd[1][3], E[3][1]);
    mfma_av<false>(ac2[3][4], wd[1][4], E[3][1]);
    wait_vm<9>();
    mfma_av<false>(ac2[3][0], wd[2][0], E[3][2]);
    v_max3(mx[0], E[3][2][0], E[3][2][1]);
    mfma_av<false>(ac2[3][1], wd[2][1], E[3][2]);
    v_max3(mx[1], E[3][2][2], E[3][2][3]);
    mfma_av<false>(ac2[3][2], wd[2][2], E[3][2]);
    mfma_av<false>(ac2[3][3], wd[2][3], E[3][2]);
    lds_write4<40960>(lr, ac2[2][0]);
    mfma_av<false>(ac2[3][4], wd[2][4], E[3][2]);
    wait_vm<8>();
    mfma_av<false>(ac2[3][0], wd[3][0], E[3][3]);
    v_max3(mx[0], E[3][3][0], E[3][3][1]);
    mfma_av<false>(ac2[3][1], wd[3][1], E[3][3]);
    v_max3(mx[1], E[3][3][2], E[3][3][3]);
    mfma_av<false>(ac2[3][2], wd[3][2], E[3][3]);
    mfma_av<false>(ac2[3][3], wd[3][3], E[3][3]);
    lds_write4<45056>(lr, ac2[2][1]);
    mfma_av<false>(ac2[3][4], wd[3][4], E[3][3]);
    wait_vm<7>();
    mfma_av<false>(ac2[3][0], wd[4][0], E[3][4]);
    v_max3(mx[0], E[3][4][0], E[3][4][1]);
    mfma_av<false>(ac2[3][1], wd[4][1], E[3][4]);
    v_max3(mx[1], E[3][4][2], E[3][4][3]);
    mfma_av<false>(ac2[3][2], wd[4][2], E[3][4]);
    mfma_av<false>(ac2[3][3], wd[4][3], E[3][4]);
    lds_write4<49152>(lr, ac2[2][2]);
    mfma_av<false>(ac2[3][4], wd[4][4], E[3][4]);
    wait_vm<6>();
    mfma_av<false>(ac2[3][0], wd[5][0], E[3][5]);
    v_max3(mx[0], E[3][5][0], E[3][5][1]);
    mfma_av<false>(ac2[3][1], wd[5][1], E[3][5]);
    v_max3(mx[1], E[3][5][2], E[3][5][3]);
    mfma_av<false>(ac2[3][2], wd[5][2], E[3][5]);
    mfma_av<false>(ac2[3][3], wd[5][3], E[3][5]);
    lds_write4<53248>(lr, ac2[2][3]);
    mfma_av<false>(ac2[3][4], wd[5][4], E[3][5]);
    wait_vm<5>();
    mfma_av<false>(ac2[3][0], wd[6][0], E[3][6]);
    v_max3(mx[0], E[3][6][0], E[3][6][1]);
    mfma_av<false>(ac2[3][1], wd[6][1], E[3][6]);
    v_max3(mx[1], E[3][6][2], E[3][6][3]);
    mfma_av<false>(ac2[3][2], wd[6][2], E[3][6]);
    mfma_av<false>(ac2[3][3], wd[6][3], E[3][6]);
    lds_write4<57344>(lr, ac2[2][4]);
    mfma_av<false>(ac2[3][4], wd[6][4], E[3][6]);
    wait_vm<4>();
    mfma_av<false>(ac2[3][0], wd[7][0], E[3][7]);
    v_max3(mx[0], E[3][7][0], E[3][7][1]);
    mfma_av<false>(ac2[3][1], wd[7][1], E[3][7]);
    v_max3(mx[1], E[3][7][2], E[3][7][3]);
    mfma_av<false>(ac2[3][2], wd[7][2], E[3][7]);
    mfma_av<false>(ac2[3][3], wd[7][3], E[3][7]);
    mfma_av<false>(ac2[3][4], wd[7][4], E[3][7]);
    wait_vm<3>();
    mfma_av<false>(ac2[3][0], wd[8][0], E[3][8]);
    v_max3(mx[0], E[3][8][0], E[3][8][1]);
    mfma_av<false>(ac2[3][1], wd[8][1], E[3][8]);
    v_max3(mx[1], E[3][8][2], E[3][8][3]);
    mfma_av<false>(ac2[3][2], wd[8][2], E[3][8]);
    mfma_av<false>(ac2[3][3], wd[8][3], E[3][8]);
    mfma_av<false>(ac2[3][4], wd[8][4], E[3][8]);
    wait_vm<2>();
    mfma_av<false>(ac2[3][0], wd[9][0], E[3][9]);
    v_max3(mx[0], E[3][9][0], E[3][9][1]);
    mfma_av<false>(ac2[3][1], wd[9][1], E[3][9]);
    v_max3(mx[1], E[3][9][2], E[3][9][3]);
    mfma_av<false>(ac2[3][2], wd[9][2], E[3][9]);
    mfma_av<false>(ac2[3][3], wd[9][3], E[3][9]);
    mfma_av<false>(ac2[3][4], wd[9][4], E[3][9]);
    wait_vm<1>();
    mfma_av<false>(ac2[3][0], wd[10][0], E[3][10]);
    v_max3(mx[0], E[3][10][0], E[3][10][1]);
    mfma_av<false>(ac2[3][1], wd[10][1], E[3][10]);
    v_max3(mx[1], E[3][10][2], E[3][10][3]);
    mfma_av<false>(ac2[3][2], wd[10][2], E[3][10]);
    mfma_av<false>(ac2[3][3], wd[10][3], E[3][10]);
    mfma_av<false>(ac2[3][4], wd[10][4], E[3][10]);
    wait_vm<0>();
    mfma_av<false>(ac2[3][0], wd[11][0], E[3][11]);
    v_max3(mx[0], E[3][11][0], E[3][11][1]);
    mfma_av<false>(ac2[3][1], wd[11][1], E[3][11]);
    v_max3(mx[1], E[3][11][2], E[3][11][3]);
    mfma_av<false>(ac2[3][2], wd[11][2], E[3][11]);
    mfma_av<false>(ac2[3][3], wd[11][3], E[3][11]);
    mfma_av<false>(ac2[3][4], wd[11][4], E[3][11]);
      bad = __builtin_amdgcn_ballot_w64(max(mx[0], mx[1]) == 0xffffffffu) != 0;
      asm volatile("s_nop 7" : "+v"(ac2[3][0]), "+v"(ac2[3][1]), "+v"(ac2[3][2]), "+v"(ac2[3][3]), "+v"(ac2[3][4])::"memory");
      static_for<0, 5>([&](auto I) { lds_write4<I * 4096>(lr + 61440, ac2[3][I]); });
    }
    BAR();
    static_for<0, 6>([&](auto J) {
      static_for<0, 4>([&](auto I) { lds_readf<I * 1024>(L[0][I], lu[J]); });
      wait_lgkm<0>();
      static_for<0, 4>([&](auto I) { tie(L[0][I]); });
      gstore2<0>(ou[J], fin(L[0][0], L[0][1], L[0][2], L[0][3], h[J]), out);
    });
  } else {
    static_for<0, 2>([&](auto J) { gstore2<0>(ou[4 + J], fin(L[J][0], L[J][1], L[J][2], L[J][3], h[4 + J]), out); });
  }
  STAMP_OUT(dbg, 2);
  asm volatile("code_end_mark:");
}

extern "C" void kernel_launch(void* const* d_in, const int* in_sizes, int n_in, void* d_out, int out_size, void* d_ws,
                              size_t ws_size, hipStream_t stream) {
  const unsigned* x = (const unsigned*)d_in[0];
  const unsigned short* wr = (const unsigned short*)d_in[1];
  const unsigned short* wi = (const unsigned short*)d_in[2];
  const u2* wg = (const u2*)d_in[4];
  const u2* wu = (const u2*)d_in[5];
  const u2* wd = (const u2*)d_in[6];
  char* ws = (char*)d_ws;
  unsigned* G1 = (unsigned*)(ws + OFF_G1);
  unsigned* SC = (unsigned*)(ws + OFF_FL);
  u4* WT = (u4*)(ws + OFF_WT);
  u4* W1F = (u4*)(ws + OFF_W1);
  u4* W2F = (u4*)(ws + OFF_W2);
  unsigned* AF = (unsigned*)(ws + OFF_AF);
  unsigned* H = (unsigned*)(ws + OFF_H);
  u4* ACT = (u4*)(ws + OFF_ACT);
  k1_kernel<<<256, 512, 0, stream>>>(x, G1, SC, wr, wi, WT, ACT DBG_PASS);
  k2_kernel<<<256, 512, 0, stream>>>(G1, SC, WT, x, H, AF, wg, wu, W1F, wd, W2F, ACT DBG_PASS);
  k4_kernel<<<256, 256, 0, stream>>>((const u4*)AF, W1F, ACT, W2F, (const u2*)H, (u2*)d_out DBG_PASS);
}
```
